```python
import torch

def _softmax(x, axis=-1):
    return torch.softmax(x, dim=axis)
def _gelu(x):
    return torch.nn.functional.gelu(x, approximate="tanh")
def _silu(x):
    return torch.nn.functional.silu(x)
def _sigmoid(x):
    return torch.sigmoid(x)
def _layernorm(x, axis=-1, eps=1e-5):
    m = x.mean(dim=axis, keepdim=True)
    v = x.var(dim=axis, keepdim=True, unbiased=False)
    return (x - m) / torch.sqrt(v + eps)
def _scatter_add(tgt, idx, src, axis=0):
    return tgt.clone().index_add_(axis, idx, src)
def _segment_sum(src, seg, n):
    out = torch.zeros((n,) + src.shape[1:], dtype=src.dtype)
    return out.index_add_(0, seg, src)
def _pad(x, pw):
    flat = tuple(v for pair in reversed(pw) for v in pair)
    return torch.nn.functional.pad(x, flat)

kernel_name = 'recurrent_scan__linear_kernel_hedgehog__geglu_ffn__prompt'

def setup_inputs():

    _w = (lambda a: (lambda t: t.to(torch.bfloat16) if t.is_floating_point() else t)(torch.as_tensor(a)))
    def _A_setup():
        B, S, D, H = 1, 2048, 512, 32
        Dh = D // H
        return {
            "x": _w((lambda *s: torch.randn(*s, dtype=torch.bfloat16) * 0.05)(B, S, D)),
            "wqkv": _w((lambda *s: torch.randn(*s, dtype=torch.bfloat16) * 0.2)(D, 3 * D)),
            "w_phi": _w((lambda *s: torch.randn(*s, dtype=torch.bfloat16) * 0.2)(H, Dh, Dh) * (Dh ** -0.5)),
            "wo": _w((lambda *s: torch.randn(*s, dtype=torch.bfloat16) * 0.05)(D, D)),
        }
    _A_in = _A_setup()

    def _B_setup():
        B, S, D, F = 1, 2048, 512, 1376
        return {
            "x": _w((lambda *s: torch.randn(*s, dtype=torch.bfloat16) * 0.05)(B, S, D)),
            "w_gate": _w((lambda *s: torch.randn(*s, dtype=torch.bfloat16) * 0.05)(D, F)),
            "w_up": _w((lambda *s: torch.randn(*s, dtype=torch.bfloat16) * 0.05)(D, F)),
            "w_down": _w((lambda *s: torch.randn(*s, dtype=torch.bfloat16) * 0.05)(F, D)),
        }
    _B_in = _B_setup()

    return {
        "A_x": _A_in["x"],
        "A_wqkv": _A_in["wqkv"],
        "A_w_phi": _A_in["w_phi"],
        "A_wo": _A_in["wo"],
        "B_x": _B_in["x"],
        "B_w_gate": _B_in["w_gate"],
        "B_w_up": _B_in["w_up"],
        "B_w_down": _B_in["w_down"],
    }

def reference(A_x, A_wqkv, A_w_phi, A_wo, B_x, B_w_gate, B_w_up, B_w_down):
    def _A_ref(x, wqkv, w_phi, wo):
        B, S, D = x.shape
        H = 32; Dh = D // H
        qkv = (lambda x, *d: x.permute(*d))((x @ wqkv).reshape(B, S, 3, H, Dh), 2, 0, 3, 1, 4)
        q, k, v = qkv[0], qkv[1], qkv[2]
        pq = torch.einsum("bhsd,hdf->bhsf", q, w_phi)
        pk = torch.einsum("bhsd,hdf->bhsf", k, w_phi)
        phi_q = torch.concatenate([_softmax(pq, axis=-1), _softmax(-pq, axis=-1)], axis=-1)
        phi_k = torch.concatenate([_softmax(pk, axis=-1), _softmax(-pk, axis=-1)], axis=-1)
        kv = torch.einsum("bhsf,bhsd->bhfd", phi_k, v)
        z  = torch.einsum("bhsf->bhf", phi_k)
        num = torch.einsum("bhqf,bhfd->bhqd", phi_q, kv)
        den = torch.einsum("bhqf,bhf->bhq", phi_q, z)[..., None] + 1e-6
        o = num / den
        return (lambda x, *d: x.permute(*d))(o, 0, 2, 1, 3).reshape(B, S, D) @ wo

    def _B_ref(x, w_gate, w_up, w_down):
        return (_gelu(x @ w_gate) * (x @ w_up)) @ w_down

    x = A_x
    xn = _layernorm(x, axis=-1)
    s = _A_ref(xn, A_wqkv, A_w_phi, A_wo)
    g = _silu(_B_ref(xn, B_w_gate, B_w_up, B_w_down))
    return x + s * g
```

```cpp
#include <hip/hip_runtime.h>
#include <hip/hip_bf16.h>

#define LAS __attribute__((address_space(3)))
#define GAS __attribute__((address_space(1)))
#define DI __device__ __forceinline__
typedef short bf16x8 __attribute__((ext_vector_type(8)));
typedef short s16x4 __attribute__((ext_vector_type(4)));
typedef float f32x16 __attribute__((ext_vector_type(16)));
typedef float f32x4 __attribute__((ext_vector_type(4)));
typedef float f32x2 __attribute__((ext_vector_type(2)));
typedef unsigned u32x4 __attribute__((ext_vector_type(4)));
typedef unsigned u32x2 __attribute__((ext_vector_type(2)));
typedef __bf16 bf2 __attribute__((ext_vector_type(2)));

constexpr int SEQ = 2048, DM = 512, FF = 1376, NH = 32;
constexpr int XB = 16384, RB = 18432, RAOFF = XB, RBOFF = XB + RB + 128, STG = XB + RB + 128 + RB;
constexpr int LDS_BYTES = 163840;
constexpr size_t WS_H = 0;
constexpr size_t WS_PQ = WS_H + (size_t)64 * 86 * 1024;
constexpr size_t WS_KVP = WS_PQ + (size_t)64 * 64 * 1024;
constexpr size_t WS_KVF = WS_KVP + (size_t)16 * 32 * 4096;
constexpr size_t WS_END = WS_KVF + 65536;

#define MFMA(a, b, c) __builtin_amdgcn_mfma_f32_32x32x16_bf16((a), (b), (c), 0, 0, 0)
#define WAITV(n) asm volatile("s_waitcnt vmcnt(" #n ")" ::: "memory")
#define WAITL0() asm volatile("s_waitcnt lgkmcnt(0)" ::: "memory")
#define BAR() __builtin_amdgcn_s_barrier()

DI unsigned pk2(float a, float b) {
  f32x2 f = {a, b};
  return __builtin_bit_cast(unsigned, __builtin_convertvector(f, bf2));
}
DI bf16x8 pack8(float a0, float a1, float a2, float a3, float a4, float a5, float a6, float a7) {
  u32x4 p = {pk2(a0, a1), pk2(a2, a3), pk2(a4, a5), pk2(a6, a7)};
  return __builtin_bit_cast(bf16x8, p);
}
template <int S0> DI bf16x8 pack_regs(const f32x16& x) {
  return pack8(x[S0], x[S0 + 1], x[S0 + 2], x[S0 + 3], x[S0 + 4], x[S0 + 5], x[S0 + 6], x[S0 + 7]);
}
DI float bf_hi(float v) { return __builtin_bit_cast(float, pk2(0.f, v) & 0xffff0000u); }
DI void glds16(const char* sbase, unsigned voff, unsigned ldst) {
  asm volatile("s_mov_b32 m0, %2\n\ts_nop 0\n\tglobal_load_lds_dwordx4 %0, %1" :: "v"(voff), "s"(sbase), "s"(ldst) : "memory");
}
#define SB() __builtin_amdgcn_sched_barrier(0)
DI bf16x8 lds_rd128(LAS char* p) { return *(LAS bf16x8*)p; }
DI bf16x8 lds_tr2(LAS char* p0, LAS char* p1) {
  s16x4 lo = __builtin_amdgcn_ds_read_tr16_b64_v4i16((LAS s16x4*)p0);
  s16x4 hi = __builtin_amdgcn_ds_read_tr16_b64_v4i16((LAS s16x4*)p1);
  return __builtin_shufflevector(lo, hi, 0, 1, 2, 3, 4, 5, 6, 7);
}
DI float dot_ones(bf16x8 v, float acc) {
  u32x4 u = __builtin_bit_cast(u32x4, v);
  const bf2 one = __builtin_bit_cast(bf2, 0x3f803f80u);
#pragma unroll
  for (int i = 0; i < 4; ++i) { const unsigned t = u[i]; acc = __builtin_amdgcn_fdot2_f32_bf16(__builtin_bit_cast(bf2, t), one, acc, false); }
  asm volatile("" : "+v"(acc));
  return acc;
}
DI float dot_self(bf16x8 v, float acc) {
  u32x4 u = __builtin_bit_cast(u32x4, v);
#pragma unroll
  for (int i = 0; i < 4; ++i) { const unsigned t = u[i]; acc = __builtin_amdgcn_fdot2_f32_bf16(__builtin_bit_cast(bf2, t), __builtin_bit_cast(bf2, t), acc, false); }
  asm volatile("" : "+v"(acc));
  return acc;
}
DI float max_x32(float v) {
  unsigned u = __builtin_bit_cast(unsigned, v);
  auto r = __builtin_amdgcn_permlane32_swap(u, u, false, false);
  const unsigned r0 = r[0], r1 = r[1];
  return fmaxf(__builtin_bit_cast(float, r0), __builtin_bit_cast(float, r1));
}
DI float add_x32(float v) {
  unsigned u = __builtin_bit_cast(unsigned, v);
  auto r = __builtin_amdgcn_permlane32_swap(u, u, false, false);
  const unsigned r0 = r[0], r1 = r[1];
  return __builtin_bit_cast(float, r0) + __builtin_bit_cast(float, r1);
}
constexpr float LOG2E = 1.4426950408889634f;

DI void dual_softmax(f32x16& p) {
#pragma unroll
  for (int g = 0; g < 2; ++g) {
    float m = fmaxf(fmaxf(fmaxf(p[8 * g], p[8 * g + 1]), fmaxf(p[8 * g + 2], p[8 * g + 3])),
                    fmaxf(fmaxf(p[8 * g + 4], p[8 * g + 5]), fmaxf(p[8 * g + 6], p[8 * g + 7])));
    m = max_x32(m);
    const float ml = -m * LOG2E;
    float s = 0.f;
#pragma unroll
    for (int i = 0; i < 8; ++i) {
      p[8 * g + i] = __builtin_amdgcn_exp2f(__builtin_fmaf(p[8 * g + i], LOG2E, ml));
      s += p[8 * g + i];
    }
    s = add_x32(s);
    const float rs = __builtin_amdgcn_rcpf(s);
#pragma unroll
    for (int i = 0; i < 8; ++i) p[8 * g + i] *= rs;
  }
}

template <bool V> struct BoolC { static constexpr bool value = V; };
struct Frags { bf16x8 x[2]; bf16x8 w[3]; };

template <bool XM> DI void load_frags(Frags& f, LAS char* xa, LAS char* wa, LAS char* we) {
  f.x[0] = lds_rd128(xa);
  f.x[1] = lds_rd128(xa + 4096);
  f.w[0] = lds_tr2(wa, wa + 1152);
  f.w[1] = lds_tr2(wa + 32, wa + 32 + 1152);
  if (XM) f.w[2] = lds_tr2(we, we + 1152);
}

struct P1State { f32x16 acc[5]; float sx[2], sxx[2], cs[3]; };

template <bool XM, bool XG>
DI void p1_mainloop(P1State& st, LAS char* lds, const char* xg, const char* wA, const char* wB, unsigned stride, int maxcc, int w, int lane) {
  const int th = w & 1, cg = w >> 1, r = lane & 31, h = lane >> 5;
  unsigned xo[2], wo[3];
#pragma unroll
  for (int a = 0; a < 2; ++a) {
    const int row = 8 * (2 * w + a) + (lane >> 3);
    xo[a] = row * 1024 + (((lane & 7) ^ ((row >> 1) & 7)) << 4);
  }
#pragma unroll
  for (int a = 0; a < 3; ++a) {
    const int p = a < 2 ? w + 8 * a : 16 + (w & 1);
    const int q = 64 * p + lane, row = q / 18, cc = min(q - row * 18, maxcc);
    wo[a] = row * stride + cc * 16;
  }
  const char* wX = (w < 6) ? wA : wB;
  const int xoffl = ((w < 6) ? RAOFF : RBOFF) + (16 + (w & 1)) * 1024;
  auto issue = [&](int s, int bufoff) {
    const char* xs = xg + s * 128;
    const unsigned ws = (unsigned)s * 64u * stride;
    const unsigned b = (unsigned)(size_t)lds + bufoff;
    glds16(xs, xo[0], b + (2 * w) * 1024);
    glds16(wA + ws, wo[0], b + RAOFF + w * 1024);
    glds16(wB + ws, wo[0], b + RBOFF + w * 1024);
    glds16(xs, xo[1], b + (2 * w + 1) * 1024);
    glds16(wA + ws, wo[1], b + RAOFF + (w + 8) * 1024);
    glds16(wB + ws, wo[1], b + RBOFF + (w + 8) * 1024);
    if (XG) glds16(wX + ws, wo[2], b + xoffl);
  };
  issue(0, 0); issue(1, STG); issue(2, 2 * STG);
  const int rot = (cg + 2 * th) & 3;
  int xad[4], wad[4];
  const int wl = (((lane >> 4) & 1) ? RBOFF : RAOFF) + (8 * h + ((lane & 15) >> 2)) * 288 + (lane & 3) * 8;
#pragma unroll
  for (int kk = 0; kk < 4; ++kk) {
    const int ks = (kk + rot) & 3;
    xad[kk] = (64 * th + r) * 128 + (((2 * ks + h) ^ ((r >> 1) & 7)) << 4);
    wad[kk] = wl + ks * 4608;
  }
  const int wsub = cg * 64, wext = 8 * 32;
#pragma unroll
  for (int i = 0; i < 5; ++i)
#pragma unroll
    for (int j = 0; j < 16; ++j) st.acc[i][j] = 0.f;
  st.sx[0] = st.sx[1] = st.sxx[0] = st.sxx[1] = st.cs[0] = st.cs[1] = st.cs[2] = 0.f;
  if (XG) WAITV(14); else WAITV(12);
  BAR();
  Frags f[2];
  load_frags<XM>(f[0], lds + xad[0], lds + wad[0] + wsub, lds + wad[0] + wext);
  int cur = 0;
  auto stage = [&](int s, auto last_tag) {
    constexpr bool LAST = decltype(last_tag)::value;
    LAS char* b = lds + cur;
    const int nxt = (cur == 2 * STG) ? 0 : cur + STG;
#pragma unroll
    for (int kk = 0; kk < 4; ++kk) {
      Frags& c = f[kk & 1];
      Frags& n = f[(kk + 1) & 1];
      if (kk < 3) {
        load_frags<XM>(n, b + xad[kk + 1], b + wad[kk + 1] + wsub, b + wad[kk + 1] + wext);
      } else if (!LAST) {
        WAITL0();
        WAITV(6);
        if (s == 6) WAITV(0);
        BAR();
        LAS char* nb = lds + nxt;
        load_frags<XM>(n, nb + xad[0], nb + wad[0] + wsub, nb + wad[0] + wext);
        SB();
        if (s < 5) issue(s + 3, cur);
      }
      SB();
      st.acc[0] = MFMA(c.w[0], c.x[0], st.acc[0]);
      st.acc[1] = MFMA(c.w[1], c.x[0], st.acc[1]);
      st.acc[2] = MFMA(c.w[0], c.x[1], st.acc[2]);
      st.acc[3] = MFMA(c.w[1], c.x[1], st.acc[3]);
      if (XM) st.acc[4] = MFMA(c.w[2], (cg & 1) ? c.x[1] : c.x[0], st.acc[4]);
      if (kk < 2) { st.cs[0] = dot_ones(c.w[0], st.cs[0]); st.cs[1] = dot_ones(c.w[1], st.cs[1]); }
      if (kk == 2) {
        st.sx[0] = dot_ones(c.x[0], st.sx[0]); st.sxx[0] = dot_self(c.x[0], st.sxx[0]);
        st.sx[1] = dot_ones(c.x[1], st.sx[1]); st.sxx[1] = dot_self(c.x[1], st.sxx[1]);
      }
      if (XM && kk == 3) st.cs[2] = dot_ones(c.w[2], st.cs[2]);
      SB();
    }
    cur = nxt;
  };
#pragma unroll 1
  for (int s = 0; s < 7; ++s) stage(s, BoolC<false>{});
  stage(7, BoolC<true>{});
}

DI float gelu_mul(float g, float u) {
  const float c1 = -2.f * 0.7978845608028654f * LOG2E, c2 = c1 * 0.044715f;
  const float e = __builtin_amdgcn_exp2f(g * __builtin_fmaf(g * g, c2, c1));
  return g * u * __builtin_amdgcn_rcpf(1.f + e);
}

__global__ __launch_bounds__(512, 2) void k_phase1(const char* __restrict__ x, const char* __restrict__ wqkv, const char* __restrict__ wphi,
                                                   const char* __restrict__ wgate, const char* __restrict__ wup, char* __restrict__ ws) {
  extern __shared__ __attribute__((aligned(16))) char smem[];
  LAS char* lds = (LAS char*)smem;
  const int lane = threadIdx.x & 63, w = __builtin_amdgcn_readfirstlane(threadIdx.x >> 6);
  const int xcd = blockIdx.x & 7, idx = blockIdx.x >> 3;
  const int ti = 8 * (xcd & 1) + (idx & 7), c = 4 * (xcd >> 1) + (idx >> 3);
  const int th = w & 1, cg = w >> 1, r = lane & 31, h = lane >> 5;
  const char *wA, *wB; unsigned stride; int maxcc, f0 = 0;
  if (c < 10) { f0 = min(144 * c, FF - 144); wA = wgate + 2 * f0; wB = wup + 2 * f0; stride = 2 * FF; maxcc = 17; }
  else if (c < 14) { wA = wqkv + 2 * (512 + 128 * (c - 10)); wB = wA + 1024; stride = 3072; maxcc = 15; }
  else { wA = wqkv + 512 * (c - 14); wB = wA + 256; stride = 3072; maxcc = 15; }
  const char* xg = x + (size_t)ti * 128 * 1024;
  P1State st;
  const bool xm = (c < 10) && (w < 4);
  if (w >= 4) p1_mainloop<false, true>(st, lds, xg, wA, wB, stride, maxcc, w, lane);
  else if (xm) p1_mainloop<true, false>(st, lds, xg, wA, wB, stride, maxcc, w, lane);
  else p1_mainloop<false, false>(st, lds, xg, wA, wB, stride, maxcc, w, lane);

  LAS float* tok = (LAS float*)lds;
  LAS float* col = (LAS float*)(lds + 4096);
  LAS char* wph = lds + 16384;
  if (c >= 10) {
    const int head0 = c < 14 ? 8 * (c - 10) : 16 * (c - 14);
    if (c >= 14 || w < 4) glds16(wphi + head0 * 512 + w * 1024, lane * 16, (unsigned)(size_t)wph + w * 1024);
  }
  {
#pragma unroll
    for (int u = 0; u < 2; ++u) {
      f32x2 v = {add_x32(st.sx[u]), add_x32(st.sxx[u])};
      if (h == 0) *(LAS f32x2*)(tok + (cg * 128 + 64 * th + 32 * u + r) * 2) = v;
    }
    const float c0 = add_x32(st.cs[0]), c1 = add_x32(st.cs[1]), c2 = add_x32(st.cs[2]);
    if (h == 0) {
      col[(th * 9 + 2 * cg) * 32 + r] = c0;
      col[(th * 9 + 2 * cg + 1) * 32 + r] = c1;
      if (xm) col[(w * 9 + 8) * 32 + r] = c2;
    }
  }
  WAITV(0);
  __syncthreads();
  float rstd[2]; bf16x8 mf[2];
#pragma unroll
  for (int u = 0; u < 2; ++u) {
    f32x2 a = {0.f, 0.f};
#pragma unroll
    for (int q = 0; q < 4; ++q) a += *(LAS f32x2*)(tok + (q * 128 + 64 * th + 32 * u + r) * 2);
    const float mean = a[0] * (1.f / DM);
    const float var = fmaxf(a[1] * (1.f / DM) - mean * mean, 0.f);
    rstd[u] = __builtin_amdgcn_rsqf(var + 1e-5f);
    const float mh = bf_hi(mean), ml = mean - mh;
    u32x4 p = {pk2(-mh, -ml), pk2(-mh, 0.f), 0u, 0u};
    if (h) p = u32x4{0u, 0u, 0u, 0u};
    mf[u] = __builtin_bit_cast(bf16x8, p);
  }
  bf16x8 cf[3];
#pragma unroll
  for (int j = 0; j < 3; ++j) {
    float cs;
    if (j < 2) cs = col[(2 * cg + j) * 32 + r] + col[(9 + 2 * cg + j) * 32 + r];
    else cs = xm ? (col[8 * 32 + r] + col[(9 + 8) * 32 + r]) + (col[(18 + 8) * 32 + r] + col[(27 + 8) * 32 + r]) : 0.f;
    const float ch = bf_hi(cs), cl = cs - ch;
    u32x4 p = {pk2(ch, ch), pk2(cl, 0.f), 0u, 0u};
    cf[j] = __builtin_bit_cast(bf16x8, p);
  }
  st.acc[0] = MFMA(cf[0], mf[0], st.acc[0]);
  st.acc[1] = MFMA(cf[1], mf[0], st.acc[1]);
  st.acc[2] = MFMA(cf[0], mf[1], st.acc[2]);
  st.acc[3] = MFMA(cf[1], mf[1], st.acc[3]);

  if (c < 10) {
    if (xm) st.acc[4] = MFMA(cf[2], (cg & 1) ? mf[1] : mf[0], st.acc[4]);
    const int jmin = (c == 9) ? 4 : 0;
#pragma unroll
    for (int i = 0; i < 5; ++i) {
      if (i == 4 && !xm) break;
      const int u = i < 4 ? (i >> 1) : (cg & 1), j = i < 4 ? 2 * cg + (i & 1) : 8;
      const float rs = i < 4 ? rstd[i >> 1] : ((cg & 1) ? rstd[1] : rstd[0]);
      float hv[8];
#pragma unroll
      for (int e = 0; e < 8; ++e) hv[e] = gelu_mul(st.acc[i][e] * rs, st.acc[i][e + 8] * rs);
      const bf16x8 o = pack8(hv[0], hv[1], hv[2], hv[3], hv[4], hv[5], hv[6], hv[7]);
      if (j >= jmin) *(GAS bf16x8*)(ws + WS_H + ((size_t)(4 * ti + 2 * th + u) * 86 + (f0 >> 4) + j) * 1024 + lane * 16) = o;
    }
  } else {
    const unsigned neg = (lane & 16) ? 0x80008000u : 0u;
    LAS char* wpl = wph + (4 * h + ((lane & 15) >> 2)) * 32 + (lane & 3) * 8;
    auto wfrag = [&](int hl) {
      u32x4 v = __builtin_bit_cast(u32x4, lds_tr2(wpl + hl * 512, wpl + hl * 512 + 256));
      v ^= neg;
      return __builtin_bit_cast(bf16x8, v);
    };
    f32x16 zero;
#pragma unroll
    for (int j = 0; j < 16; ++j) zero[j] = 0.f;
    if (c >= 14) {
      const int head0 = 16 * (c - 14);
#pragma unroll
      for (int jj = 0; jj < 2; ++jj)
#pragma unroll
        for (int ab = 0; ab < 2; ++ab) {
          const int hl = 8 * ab + 2 * cg + jj;
          const bf16x8 wf = wfrag(hl);
#pragma unroll
          for (int u = 0; u < 2; ++u) {
            const f32x16& a = st.acc[2 * u + jj];
            const float rs = rstd[u];
            const bf16x8 qf = pack8(a[8 * ab] * rs, a[8 * ab + 1] * rs, a[8 * ab + 2] * rs, a[8 * ab + 3] * rs,
                                    a[8 * ab + 4] * rs, a[8 * ab + 5] * rs, a[8 * ab + 6] * rs, a[8 * ab + 7] * rs);
            f32x16 p = MFMA(wf, qf, zero);
            dual_softmax(p);
            char* dst = ws + WS_PQ + ((size_t)(4 * ti + 2 * th + u) * 32 + head0 + hl) * 2048 + lane * 16;
            *(GAS bf16x8*)dst = pack_regs<0>(p);
            *(GAS bf16x8*)(dst + 1024) = pack_regs<8>(p);
          }
        }
    } else {
      bf16x8 idf[2];
#pragma unroll
      for (int s = 0; s < 2; ++s)
#pragma unroll
        for (int j = 0; j < 8; ++j) idf[s][j] = (16 * s + 8 * (j >> 2) + 4 * h + (j & 3) == r) ? (short)0x3f80 : (short)0;
      f32x16 ones;
#pragma unroll
      for (int j = 0; j < 16; ++j) ones[j] = (r >= 16) ? 1.f : 0.f;
      f32x16 kv[2];
#pragma unroll
      for (int jj = 0; jj < 2; ++jj) {
        const bf16x8 wf = wfrag(2 * cg + jj);
        kv[jj] = zero;
#pragma unroll
        for (int u = 0; u < 2; ++u) {
          const f32x16& a = st.acc[2 * u + jj];
          const float rs = rstd[u];
          const bf16x8 kf = pack8(a[0] * rs, a[1] * rs, a[2] * rs, a[3] * rs, a[4] * rs, a[5] * rs, a[6] * rs, a[7] * rs);
          const bf16x8 vf = pack8(a[8] * rs, a[9] * rs, a[10] * rs, a[11] * rs, a[12] * rs, a[13] * rs, a[14] * rs, a[15] * rs);
          f32x16 p = MFMA(wf, kf, zero);
          dual_softmax(p);
          f32x16 pt = MFMA(pack_regs<0>(p), idf[0], zero);
          pt = MFMA(pack_regs<8>(p), idf[1], pt);
          const f32x16 vt = MFMA(vf, idf[0], ones);
          kv[jj] = MFMA(pack_regs<0>(pt), pack_regs<0>(vt), kv[jj]);
          kv[jj] = MFMA(pack_regs<8>(pt), pack_regs<8>(vt), kv[jj]);
        }
      }
      LAS f32x4* ex = (LAS f32x4*)(lds + 32768);
      const f32x16& give = th ? kv[0] : kv[1];
#pragma unroll
      for (int q = 0; q < 4; ++q) ex[(w * 4 + q) * 64 + lane] = f32x4{give[4 * q], give[4 * q + 1], give[4 * q + 2], give[4 * q + 3]};
      __syncthreads();
      const f32x16& keep = th ? kv[1] : kv[0];
      const int head = 8 * (c - 10) + 2 * cg + th;
      char* dst = ws + WS_KVP + ((size_t)ti * 32 + head) * 4096 + lane * 64;
      if (r <= 16) {
#pragma unroll
        for (int q = 0; q < 4; ++q) {
          const f32x4 o = ex[((w ^ 1) * 4 + q) * 64 + lane];
          *(GAS f32x4*)(dst + q * 16) = f32x4{keep[4 * q] + o[0], keep[4 * q + 1] + o[1], keep[4 * q + 2] + o[2], keep[4 * q + 3] + o[3]};
        }
      }
    }
  }
}

__global__ __launch_bounds__(256) void k_reduce(char* __restrict__ ws) {
  const int head = blockIdx.x, lane = threadIdx.x & 63, qd = threadIdx.x >> 6;
  const int sl = (lane & 31) > 16 ? (lane & 32) + 16 : lane;
  const char* src = ws + WS_KVP + (size_t)head * 4096 + sl * 64 + qd * 16;
  f32x4 a = {0.f, 0.f, 0.f, 0.f};
#pragma unroll
  for (int t = 0; t < 16; ++t) a += *(const GAS f32x4*)(src + (size_t)t * 32 * 4096);
  u32x2 o = {pk2(a[0], a[1]), pk2(a[2], a[3])};
  *(GAS u32x2*)(ws + WS_KVF + (head * 2 + (qd >> 1)) * 1024 + lane * 16 + (qd & 1) * 8) = o;
}

constexpr int P2_WO = 0, P2_WD = 65536, P2_WDH = 23 * 2048;
constexpr int NA = 20;

__global__ __launch_bounds__(512, 2) void k_phase2(const char* __restrict__ x, const char* __restrict__ wo, const char* __restrict__ wdown,
                                                   const char* __restrict__ ws, char* __restrict__ out) {
  extern __shared__ __attribute__((aligned(16))) char smem[];
  LAS char* lds = (LAS char*)smem;
  const int lane = threadIdx.x & 63, w = __builtin_amdgcn_readfirstlane(threadIdx.x >> 6);
  const int xcd = blockIdx.x & 7, idx = blockIdx.x >> 3;
  const int ti = 4 * xcd + (idx & 3), cj = idx >> 2;
  const int tg = w & 1, kh = (w >> 1) & 1, h = lane >> 5;
  const unsigned wl = (lane >> 3) * 1024 + cj * 128 + ((((lane & 7) ^ (((lane >> 4) & 1) << 2))) << 4);
  const int q = (lane & 15) >> 2, p = lane & 3, g = (lane >> 4) & 1;
  int tro[2];
#pragma unroll
  for (int cc = 0; cc < 2; ++cc) tro[cc] = (4 * h + q) * 128 + (((4 * cc + 2 * g + (p >> 1)) ^ ((q >> 1) << 2)) << 4) + (p & 1) * 8;
  f32x16 acc[2];
#pragma unroll
  for (int j = 0; j < 16; ++j) acc[0][j] = acc[1][j] = 0.f;
  const size_t xoff = ((size_t)ti * 64 + (threadIdx.x >> 3)) * 1024 + cj * 128 + (threadIdx.x & 7) * 16;

  if (w < 4) {
    const char* wd = wdown + (size_t)(43 * kh) * 16 * 1024;
    LAS char* img = lds + P2_WD + kh * P2_WDH;
#pragma unroll
    for (int i = 0; i < 23; ++i) glds16(wd + (size_t)(2 * i + tg) * 8192, wl, (unsigned)(size_t)img + (2 * i + tg) * 1024);
    u32x4 wr[20];
#pragma unroll
    for (int i = 0; i < 20; ++i) wr[i] = *(const GAS u32x4*)(wd + (size_t)(46 + 2 * i + tg) * 8192 + wl);
    const char* hp = ws + WS_H + ((size_t)(2 * ti + tg) * 86 + 43 * kh) * 1024 + lane * 16;
    bf16x8 a[NA];
#pragma unroll
    for (int i = 0; i < NA; ++i) a[i] = *(const GAS bf16x8*)(hp + i * 1024);
    SB();
    WAITV(40);
    BAR();
#pragma unroll
    for (int i = 0; i < 43; ++i) {
      if (i == 20) {
        WAITL0(); BAR();
#pragma unroll
        for (int k = 0; k < 20; ++k) *(LAS u32x4*)(img + (2 * k + tg) * 1024 + lane * 16) = wr[k];
      }
      if (i == 23) { WAITL0(); BAR(); }
      const int slot = i < 23 ? i : i - 23;
      const bf16x8 w0 = lds_tr2(img + slot * 2048 + tro[0], img + slot * 2048 + tro[0] + 1024);
      const bf16x8 w1 = lds_tr2(img + slot * 2048 + tro[1], img + slot * 2048 + tro[1] + 1024);
      const bf16x8 cur = a[i % NA];
      if (i + NA < 43) a[i % NA] = *(const GAS bf16x8*)(hp + (i + NA) * 1024);
      acc[0] = MFMA(w0, cur, acc[0]);
      acc[1] = MFMA(w1, cur, acc[1]);
      SB();
    }
  } else {
    const char* wp = wo + (size_t)(16 * kh) * 16 * 1024;
    LAS char* img = lds + P2_WO + kh * 32768;
#pragma unroll
    for (int i = 0; i < 16; ++i) glds16(wp + (size_t)(2 * i + tg) * 8192, wl, (unsigned)(size_t)img + (2 * i + tg) * 1024);
    const char* pq = ws + WS_PQ + ((size_t)(2 * ti + tg) * 32 + 16 * kh) * 2048 + lane * 16;
    const char* kvp = ws + WS_KVF + (size_t)(16 * kh) * 2048 + lane * 16;
    bf16x8 ph[16][2];
#pragma unroll
    for (int i = 0; i < 16; ++i) { ph[i][0] = *(const GAS bf16x8*)(pq + i * 2048); ph[i][1] = *(const GAS bf16x8*)(pq + i * 2048 + 1024); }
    bf16x8 kv[4][2];
#pragma unroll
    for (int i = 0; i < 4; ++i) { kv[i][0] = *(const GAS bf16x8*)(kvp + i * 2048); kv[i][1] = *(const GAS bf16x8*)(kvp + i * 2048 + 1024); }
    SB();
    WAITV(40);
    BAR(); BAR(); BAR();
    SB();
    f32x16 zero;
#pragma unroll
    for (int j = 0; j < 16; ++j) zero[j] = 0.f;
#pragma unroll
    for (int i = 0; i < 16; ++i) {
      f32x16 nm = MFMA(kv[i & 3][0], ph[i][0], zero);
      nm = MFMA(kv[i & 3][1], ph[i][1], nm);
      if (i + 4 < 16) { kv[i & 3][0] = *(const GAS bf16x8*)(kvp + (i + 4) * 2048); kv[i & 3][1] = *(const GAS bf16x8*)(kvp + (i + 4) * 2048 + 1024); }
      const float rd = __builtin_amdgcn_rcpf(nm[8] + 1e-6f);
      const bf16x8 of = pack8(nm[0] * rd, nm[1] * rd, nm[2] * rd, nm[3] * rd, nm[4] * rd, nm[5] * rd, nm[6] * rd, nm[7] * rd);
      const bf16x8 w0 = lds_tr2(img + i * 2048 + tro[0], img + i * 2048 + tro[0] + 1024);
      const bf16x8 w1 = lds_tr2(img + i * 2048 + tro[1], img + i * 2048 + tro[1] + 1024);
      acc[0] = MFMA(w0, of, acc[0]);
      acc[1] = MFMA(w1, of, acc[1]);
      SB();
    }
  }
  const u32x4 xr = *(const GAS u32x4*)(x + xoff);
  __syncthreads();
  {
    const int part = w >> 1;
    LAS char* row = lds + (part * 64 + tg * 32 + (lane & 31)) * 272 + h * 16;
#pragma unroll
    for (int cc = 0; cc < 2; ++cc)
#pragma unroll
      for (int k = 0; k < 4; ++k)
        *(LAS f32x4*)(row + cc * 128 + k * 32) = f32x4{acc[cc][4 * k], acc[cc][4 * k + 1], acc[cc][4 * k + 2], acc[cc][4 * k + 3]};
  }
  __syncthreads();
  {
    LAS char* rp = lds + (threadIdx.x >> 3) * 272 + (threadIdx.x & 7) * 32;
    float res[8];
#pragma unroll
    for (int hf = 0; hf < 2; ++hf) {
      const f32x4 gg = *(LAS f32x4*)(rp + hf * 16) + *(LAS f32x4*)(rp + 64 * 272 + hf * 16);
      const f32x4 ss = *(LAS f32x4*)(rp + 128 * 272 + hf * 16) + *(LAS f32x4*)(rp + 192 * 272 + hf * 16);
#pragma unroll
      for (int e = 0; e < 4; ++e) {
        const unsigned xw = xr[hf * 2 + (e >> 1)];
        const float xv = __builtin_bit_cast(float, (e & 1) ? (xw & 0xffff0000u) : (xw << 16));
        const float sg = gg[e] * __builtin_amdgcn_rcpf(1.f + __builtin_amdgcn_exp2f(-gg[e] * LOG2E));
        res[hf * 4 + e] = xv + ss[e] * sg;
      }
    }
    *(GAS bf16x8*)(out + xoff) = pack8(res[0], res[1], res[2], res[3], res[4], res[5], res[6], res[7]);
  }
}

extern "C" void kernel_launch(void* const* d_in, const int* in_sizes, int n_in, void* d_out, int out_size, void* d_ws, size_t ws_size,
                              hipStream_t stream) {
  const char* x = (const char*)d_in[0];
  const char* wqkv = (const char*)d_in[1];
  const char* wphi = (const char*)d_in[2];
  const char* wo = (const char*)d_in[3];
  const char* wg = (const char*)d_in[5];
  const char* wu = (const char*)d_in[6];
  const char* wd = (const char*)d_in[7];
  (void)hipFuncSetAttribute((const void*)k_phase1, hipFuncAttributeMaxDynamicSharedMemorySize, LDS_BYTES);
  (void)hipFuncSetAttribute((const void*)k_phase2, hipFuncAttributeMaxDynamicSharedMemorySize, LDS_BYTES);
  k_phase1<<<256, 512, LDS_BYTES, stream>>>(x, wqkv, wphi, wg, wu, (char*)d_ws);
  k_reduce<<<32, 256, 0, stream>>>((char*)d_ws);
  k_phase2<<<256, 512, LDS_BYTES, stream>>>(x, wo, wd, (const char*)d_ws, (char*)d_out);
}
```

```cpp
#include <hip/hip_runtime.h>
#include <hip/hip_bf16.h>

#define LAS __attribute__((address_space(3)))
#define GAS __attribute__((address_space(1)))
#define DI __device__ __forceinline__
typedef short bf16x8 __attribute__((ext_vector_type(8)));
typedef short s16x4 __attribute__((ext_vector_type(4)));
typedef float f32x16 __attribute__((ext_vector_type(16)));
typedef float f32x4 __attribute__((ext_vector_type(4)));
typedef float f32x2 __attribute__((ext_vector_type(2)));
typedef unsigned u32x4 __attribute__((ext_vector_type(4)));
typedef unsigned u32x2 __attribute__((ext_vector_type(2)));
typedef __bf16 bf2 __attribute__((ext_vector_type(2)));
typedef unsigned long long u64;

constexpr int DM = 512, FF = 1376;
constexpr int XB = 16384, RG = 12288, GOFF = XB, UOFF = XB + RG + 32, QOFF = XB + 2 * RG + 32, STG = XB + 3 * RG + 32;
constexpr int L_WPH = 3 * STG, L_CNT = L_WPH + 1024, L_COL = L_CNT + 128;
constexpr int L_SCR = 65536, SCR_BYTES = 20992, L_KVF = L_SCR + 16384;
constexpr int LDS_BYTES = 163840;
constexpr int L_DUMMY = L_COL + 2560;
static_assert(L_DUMMY + 256 <= LDS_BYTES, "lds");
enum { C_KV = 0, C_H = 1, C_O = 2, C_PQ = 3, C_EX = 4  , C_R1 = 8  , C_R2 = 16   };
constexpr unsigned FS = 256;
constexpr unsigned WS_ST = 4096, WS_FKV = 65536, WS_FH = 2 * 65536, WS_FO = 3 * 65536;
constexpr unsigned WS_H = 4 * 65536;
constexpr unsigned WS_O = WS_H + 64u * 86 * 1024;
constexpr unsigned WS_KVP = WS_O + 64u * 32 * 1024;
constexpr unsigned WS_END = WS_KVP + 512u * 2176;
#define PSLEEP 2
constexpr int SC1 = 16;

#define NO_WARM2
#define WARM2_AT 2
constexpr unsigned SPIN_G = 1u << 15, SPIN_L = 1u << 19;

#define STAMP(i) do {} while (0)
#define DIAG_BEGIN() do {} while (0)
#define DIAG_END() do {} while (0)
#define DGARG
#define DGPASS

#define XW12 "13"
#define XW14 "15"
#define MFMA(a, b, c) __builtin_amdgcn_mfma_f32_32x32x16_bf16((a), (b), (c), 0, 0, 0)
#define WAITV(n) asm volatile("s_waitcnt vmcnt(" #n ")" ::: "memory")
#define WAITL0() asm volatile("s_waitcnt lgkmcnt(0)" ::: "memory")
#define BAR() __builtin_amdgcn_s_barrier()
#define SB() __builtin_amdgcn_sched_barrier(0)
#define RLX_AGENT __ATOMIC_RELAXED, __HIP_MEMORY_SCOPE_AGENT
#define RLX_WG __ATOMIC_RELAXED, __HIP_MEMORY_SCOPE_WORKGROUP

DI float asf(unsigned u) { return __builtin_bit_cast(float, u); }
DI unsigned asu(float f) { return __builtin_bit_cast(unsigned, f); }
DI unsigned pk2(float a, float b) {
  f32x2 f = {a, b};
  return __builtin_bit_cast(unsigned, __builtin_convertvector(f, bf2));
}
DI bf16x8 pack8(float a0, float a1, float a2, float a3, float a4, float a5, float a6, float a7) {
  u32x4 p = {pk2(a0, a1), pk2(a2, a3), pk2(a4, a5), pk2(a6, a7)};
  return __builtin_bit_cast(bf16x8, p);
}
template <int S0> DI bf16x8 pack_regs(const f32x16& x) {
  return pack8(x[S0], x[S0 + 1], x[S0 + 2], x[S0 + 3], x[S0 + 4], x[S0 + 5], x[S0 + 6], x[S0 + 7]);
}
template <int S0> DI bf16x8 pack_regs_scaled(const f32x16& x, float s) {
  return pack8(x[S0] * s, x[S0 + 1] * s, x[S0 + 2] * s, x[S0 + 3] * s, x[S0 + 4] * s, x[S0 + 5] * s, x[S0 + 6] * s, x[S0 + 7] * s);
}
DI float bf_hi(float v) { return asf(pk2(0.f, v) & 0xffff0000u); }
DI void glds16(const char* sbase, unsigned voff, unsigned ldst) {
  asm volatile("s_mov_b32 m0, %2\n\ts_nop 0\n\tglobal_load_lds_dwordx4 %0, %1" ::"v"(voff), "s"(sbase), "s"(ldst) : "memory");
}
DI void warm(const char* lane_ptr, unsigned ldst) {
  asm volatile("s_mov_b32 m0, %1\n\ts_nop 0\n\tglobal_load_lds_dword %0, off" ::"v"(lane_ptr), "s"(ldst) : "memory");
}
DI bf16x8 lds_rd128(LAS char* p) { return *(LAS bf16x8*)p; }
DI bf16x8 lds_tr2(LAS char* p0, LAS char* p1) {
  s16x4 lo = __builtin_amdgcn_ds_read_tr16_b64_v4i16((LAS s16x4*)p0);
  s16x4 hi = __builtin_amdgcn_ds_read_tr16_b64_v4i16((LAS s16x4*)p1);
  return __builtin_shufflevector(lo, hi, 0, 1, 2, 3, 4, 5, 6, 7);
}
DI void acc_pair(unsigned u, float& a0, float& a1) {
  unsigned t0, t1;
  asm volatile("v_lshlrev_b32 %2, 16, %4\n\tv_and_b32 %3, %5, %4\n\tv_add_f32 %0, %0, %2\n\tv_add_f32 %1, %1, %3"
               : "+v"(a0), "+v"(a1), "=&v"(t0), "=&v"(t1) : "v"(u), "s"(0xffff0000u));
}
#define MX(a, b) __builtin_elementwise_maximum((a), (b))
DI float max_x32(float v) {
  auto r = __builtin_amdgcn_permlane32_swap(asu(v), asu(v), false, false);
  const unsigned r0 = r[0], r1 = r[1];
  return MX(asf(r0), asf(r1));
}
DI float add_x32(float v) {
  auto r = __builtin_amdgcn_permlane32_swap(asu(v), asu(v), false, false);
  const unsigned r0 = r[0], r1 = r[1];
  return asf(r0) + asf(r1);
}
template <int CTRL> DI float dpp_add(float v) { return v + asf(__builtin_amdgcn_update_dpp(0u, asu(v), CTRL, 0xf, 0xf, true)); }
DI float wave_sum(float v) {
  v = dpp_add<0xB1>(v); v = dpp_add<0x4E>(v); v = dpp_add<0x141>(v); v = dpp_add<0x140>(v);
  auto r = __builtin_amdgcn_permlane16_swap(asu(v), asu(v), false, false);
  const unsigned r0 = r[0], r1 = r[1];
  return add_x32(asf(r0) + asf(r1));
}
constexpr float LOG2E = 1.4426950408889634f;

DI void dual_softmax(f32x16& p) {
  float m0 = MX(MX(MX(MX(p[0], p[1]), p[2]), MX(MX(p[3], p[4]), p[5])), MX(p[6], p[7]));
  float m1 = MX(MX(MX(MX(p[8], p[9]), p[10]), MX(MX(p[11], p[12]), p[13])), MX(p[14], p[15]));
  m0 = max_x32(m0) * -LOG2E;
  m1 = max_x32(m1) * -LOG2E;
#pragma unroll
  for (int i = 0; i < 8; ++i) {
    p[i] = __builtin_amdgcn_exp2f(__builtin_fmaf(p[i], LOG2E, m0));
    p[8 + i] = __builtin_amdgcn_exp2f(__builtin_fmaf(p[8 + i], LOG2E, m1));
  }
  const float s0 = add_x32(((p[0] + p[1]) + (p[2] + p[3])) + ((p[4] + p[5]) + (p[6] + p[7])));
  const float s1 = add_x32(((p[8] + p[9]) + (p[10] + p[11])) + ((p[12] + p[13]) + (p[14] + p[15])));
  const float r0 = __builtin_amdgcn_rcpf(s0), r1 = __builtin_amdgcn_rcpf(s1);
#pragma unroll
  for (int i = 0; i < 8; ++i) { p[i] *= r0; p[8 + i] *= r1; }
}
DI u32x4 geglu8(const f32x16& acc, float rs) {
  const float c1 = -2.f * 0.7978845608028654f * LOG2E, c2 = c1 * 0.044715f;
  const float k1 = c1 * rs, rs2 = rs * rs, k2 = c2 * rs2 * rs, ir2 = __builtin_amdgcn_rcpf(rs2);
  float hv[8];
#pragma unroll
  for (int e = 0; e < 8; ++e) {
    const float a = acc[e];
    const float ex = __builtin_amdgcn_exp2f(a * __builtin_fmaf(a * a, k2, k1));
    hv[e] = (a * acc[e + 8]) * __builtin_amdgcn_rcpf(__builtin_fmaf(ex, ir2, ir2));
  }
  return u32x4{pk2(hv[0], hv[1]), pk2(hv[2], hv[3]), pk2(hv[4], hv[5]), pk2(hv[6], hv[7])};
}
DI void lds_wait_ge(LAS char* lds, int idx, unsigned want) {
  volatile LAS unsigned* p = (volatile LAS unsigned*)(lds + L_CNT) + idx;
#pragma unroll 1
  for (unsigned s = 0; s < SPIN_L; ++s) {
    if (__builtin_amdgcn_readfirstlane(*p) >= want) break;
    __builtin_amdgcn_s_sleep(1);
  }
  asm volatile("" ::: "memory");
}
DI unsigned lds_arrive(LAS char* lds, int idx, int lane) {
  unsigned old = 0;
  if (lane == 0) old = __hip_atomic_fetch_add((LAS unsigned*)(lds + L_CNT) + idx, 1u, RLX_WG);
  return __builtin_amdgcn_readfirstlane(old);
}
DI void publish(LAS char* lds, int idx, unsigned n, char* ws, unsigned flag_off, u64 tag64, int lane) {
  if (lds_arrive(lds, idx, lane) == n - 1 && lane == 0) __hip_atomic_store((GAS u64*)(ws + flag_off), tag64, RLX_AGENT);
}
DI void poll16(const char* ws, unsigned off, int lo, int hi, u64 tag64, int lane) {
  const int l = min(max(lane & 15, lo), hi);
  GAS u64* p = (GAS u64*)(ws + off + l * FS);
#pragma unroll 1
  for (unsigned s = 0; s < SPIN_G; ++s) {
    const u64 v = __hip_atomic_load(p, RLX_AGENT);
    if (__all(v == tag64)) break;
    __builtin_amdgcn_s_sleep(PSLEEP);
  }
  __builtin_amdgcn_fence(__ATOMIC_ACQUIRE, "wavefront");
}

template <bool V> struct BoolC { static constexpr bool value = V; };
template <int V> struct IntC { static constexpr int value = V; };
struct Frags { bf16x8 x[2]; bf16x8 w[3]; };
struct P1State { f32x16 acc[5]; float cs[3][2]; float rstd[2]; bf16x8 mf[2]; unsigned tag; };

DI unsigned slot_lds(int G) { return 2048u * G + (G >= 32 ? (unsigned)SCR_BYTES : 0u); }
DI const char* slot_src(int G, const char* wdown, const char* wo) {
  return G < 48 ? wdown + (size_t)(G + (G >= 16 ? 13 : 0) + (G >= 32 ? 12 : 0)) * 16384 : wo + (size_t)(G - 48) * 16384;
}

template <bool LO>
DI void p1_mainloop(P1State& st, LAS char* lds, const char* xg, const char* wG, const char* wU, const char* wQ, const char* wdown, const char* wo,
                    char* ws, int ti, int c, unsigned wl, int w, int lane, const char* warm1, const char* warm2 DGARG) {
  const int th = w & 1, cg = w >> 1, r = lane & 31, h = lane >> 5;
  const unsigned lb = (unsigned)(size_t)lds;
  u32x4 xrow;
  const int myrow = 8 * c + w;
  asm volatile("global_load_dwordx4 %0, %1, %2" : "=v"(xrow) : "v"(myrow * 1024 + lane * 16), "s"(xg) : "memory");
  unsigned xo[2], go[2], qo[2];
#pragma unroll
  for (int a = 0; a < 2; ++a) {
    const int row = 8 * (2 * w + a) + (lane >> 3);
    xo[a] = row * 1024 + (((lane & 7) ^ ((row >> 1) & 7)) << 4);
    const int p = a == 0 ? w : 8 + (w & 3);
    const int q = 64 * p + lane, rr = q / 12, cc = q - rr * 12;
    go[a] = rr * (2 * FF) + cc * 16;
    const int mat = cc < 8 ? 1 + ((cc >> 1) & 1) : 0, hh = cc < 8 ? cc >> 2 : (cc - 8) >> 1;
    qo[a] = rr * 3072 + mat * 1024 + hh * 32 + (cc & 1) * 16;
  }
  const int p2 = 8 + (w & 3);
  auto piece = [&](int k, int s, unsigned bo) {
    const unsigned b = lb + bo;
    if (k == 0) glds16(xg + s * 128, xo[0], b + 2 * w * 1024);
    if (k == 1) glds16(wG + s * (128 * FF), go[0], b + GOFF + w * 1024);
    if (k == 2) glds16(wU + s * (128 * FF), go[0], b + UOFF + w * 1024);
    if (k == 3) glds16(wQ + s * (64 * 3072), qo[0], b + QOFF + w * 1024);
    if (k == 4) glds16(xg + s * 128, xo[1], b + (2 * w + 1) * 1024);
    if (k == 5) glds16((LO ? wG : wU) + s * (128 * FF), go[1], b + (LO ? GOFF : UOFF) + p2 * 1024);
    if (k == 6 && !LO) glds16(wQ + s * (64 * 3072), qo[1], b + QOFF + p2 * 1024);
  };
  auto w2piece = [&](int k, int G0, int nslot) {
    const int L = (w >> 1) + 4 * k;
    if (4 * k + 3 < nslot || L < nslot) glds16(slot_src(G0 + L, wdown, wo) + th * 8192, wl, lb + slot_lds(G0 + L) + th * 1024);
  };
#pragma unroll
  for (int k = 0; k < 7; ++k) piece(k, 0, 0);
  BAR();
#pragma unroll
  for (int k = 0; k < 7; ++k) piece(k, 1, STG);
  warm(warm1, lb + L_DUMMY);
  unsigned tag;
  asm volatile("s_load_dword %0, %1, 0x0" : "=s"(tag) : "s"(ws) : "memory");
  const int rot = LO ? 2 * th + cg : 2 * th;
  const int g = (lane >> 4) & 1, u0 = LO ? cg : 0;
  const int wrow = (8 * h + ((lane & 15) >> 2)) * 192 + (lane & 3) * 8;
  const int wFF = (g ? UOFF : GOFF) + wrow + (LO ? 64 * cg : 32 * (cg + 2));
  const int wQK = QOFF + wrow + g * 32 + (LO ? 128 : 64 * (cg - 2));
  int xad[4], kof[4];
#pragma unroll
  for (int kk = 0; kk < 4; ++kk) {
    const int ks = (kk + rot) & 3;
    xad[kk] = (64 * th + r) * 128 + (((2 * ks + h) ^ ((r >> 1) & 7)) << 4);
    kof[kk] = ks * 3072;
  }
  const int xa0 = u0 * 4096, xa1 = 4096 - xa0;
  auto load_frags = [&](Frags& f, int bo, int kk) {
    LAS char* xa = lds + bo + xad[kk];
    LAS char* a = lds + (bo + kof[kk]) + wFF;
    LAS char* q = lds + (bo + kof[kk]) + wQK;
    f.x[0] = lds_rd128(xa + xa0);
    if (LO) { f.w[0] = lds_tr2(a, a + 768); f.w[1] = lds_tr2(a + 32, a + 32 + 768); }
    else { f.w[0] = lds_tr2(q, q + 768); f.w[1] = lds_tr2(a, a + 768); }
    f.x[1] = lds_rd128(xa + xa1);
    if (LO) f.w[2] = lds_tr2(q, q + 768);
  };
#pragma unroll
  for (int i = 0; i < 5; ++i)
#pragma unroll
    for (int j = 0; j < 16; ++j) st.acc[i][j] = 0.f;
#pragma unroll
  for (int i = 0; i < 3; ++i) st.cs[i][0] = st.cs[i][1] = 0.f;
  if (LO) asm volatile("s_waitcnt vmcnt(" XW12 ") lgkmcnt(0)" : "+v"(xrow), "+s"(tag)::"memory");
  else asm volatile("s_waitcnt vmcnt(" XW14 ") lgkmcnt(0)" : "+v"(xrow), "+s"(tag)::"memory");
  tag += 1u;
  st.tag = tag;
  {
    float v[8];
#pragma unroll
    for (int i = 0; i < 4; ++i) { const unsigned t = xrow[i]; v[2 * i] = asf(t << 16); v[2 * i + 1] = asf(t & 0xffff0000u); }
    const float mean = wave_sum(((v[0] + v[1]) + (v[2] + v[3])) + ((v[4] + v[5]) + (v[6] + v[7]))) * (1.f / DM);
    float ss = 0.f;
#pragma unroll
    for (int i = 0; i < 8; ++i) { const float d = v[i] - mean; ss = __builtin_fmaf(d, d, ss); }
    const float rstd = __builtin_amdgcn_rsqf(wave_sum(ss) * (1.f / DM) + 1e-5f);
    const u32x4 gr = {asu(mean), tag, asu(rstd), tag};
    if (lane == 0)
      asm volatile("global_store_dwordx4 %0, %1, %2 sc1\n\ts_nop 1" ::"v"(WS_ST + (ti * 128 + myrow) * 16), "v"(gr), "s"(ws) : "memory");
  }
  STAMP(1);
  Frags f[2];
  u32x4 stt[2];
  auto mma = [&](const Frags& cf, auto kk_tag, auto&& dma0, auto&& dma1) {
    constexpr int kk = decltype(kk_tag)::value;
    const u32x4 cw = __builtin_bit_cast(u32x4, cf.w[kk < 2 ? 0 : 1]);
    float(&cs)[2] = st.cs[kk < 2 ? 0 : 1];
    SB();
    st.acc[0] = MFMA(cf.w[0], cf.x[0], st.acc[0]); acc_pair(cw[0], cs[0], cs[1]); SB();
    st.acc[1] = MFMA(cf.w[1], cf.x[0], st.acc[1]); acc_pair(cw[1], cs[0], cs[1]); dma0(); SB();
    st.acc[2] = MFMA(cf.w[0], cf.x[1], st.acc[2]); acc_pair(cw[2], cs[0], cs[1]); SB();
    st.acc[3] = MFMA(cf.w[1], cf.x[1], st.acc[3]); acc_pair(cw[3], cs[0], cs[1]); dma1(); SB();
    if (LO) {
      st.acc[4] = MFMA(cf.w[2], cf.x[0], st.acc[4]);
      if (kk == 0) {
        const u32x4 cq = __builtin_bit_cast(u32x4, cf.w[2]);
#pragma unroll
        for (int i = 0; i < 4; ++i) acc_pair(cq[i], st.cs[2][0], st.cs[2][1]);
      }
      SB();
    }
  };
  int cur = 0;
  auto iter = [&](int s, auto mode_tag) {
    constexpr int MODE = decltype(mode_tag)::value;
    const int fre = (cur == 0) ? 2 * STG : cur - STG;
    auto dma = [&](int k) {
      if (MODE <= 1) piece(k, s + 2, fre);
      if (MODE == 2) w2piece(k, 42, 25);
      if (MODE == 3) w2piece(k, 0, 26);
    };
    WAITL0();
    if (MODE == 3) asm volatile("s_waitcnt vmcnt(6)" : "+v"(stt[0]), "+v"(stt[1])::"memory"); else WAITV(6);
    BAR();
    if (MODE == 0) STAMP(2);
    load_frags(f[0], cur, 0);
    if (MODE == 3) {
#pragma unroll 1
      for (unsigned sp = 0; sp < SPIN_G; ++sp) {
        if (__all((((stt[0][1] ^ tag) | (stt[0][3] ^ tag)) | ((stt[1][1] ^ tag) | (stt[1][3] ^ tag))) == 0u)) break;
        asm volatile("global_load_dwordx4 %0, %2, %4 sc1\n\tglobal_load_dwordx4 %1, %3, %4 sc1\n\ts_waitcnt vmcnt(0)"
                     : "=&v"(stt[0]), "=&v"(stt[1])
                     : "v"(WS_ST + (ti * 128 + 64 * th + 32 * u0 + r) * 16), "v"(WS_ST + (ti * 128 + 64 * th + 32 * (1 ^ u0) + r) * 16), "s"(ws)
                     : "memory");
      }
#pragma unroll
      for (int u = 0; u < 2; ++u) {
        const unsigned mu = stt[u][0], ru = stt[u][2];
        const float mean = h ? 0.f : -asf(mu);
        st.rstd[u] = asf(ru);
        const float mh = bf_hi(mean);
        st.mf[u] = __builtin_bit_cast(bf16x8, u32x4{pk2(mh, mean - mh), pk2(mh, 0.f), 0u, 0u});
      }
    }
    if (MODE == 2) {
#pragma unroll
      for (int u = 0; u < 2; ++u)
        asm volatile("global_load_dwordx4 %0, %1, %2 sc1" : "=v"(stt[u]) : "v"(WS_ST + (ti * 128 + 64 * th + 32 * (u ^ u0) + r) * 16), "s"(ws) : "memory");
    }
    if (MODE != 0) mma(f[1], IntC<3>{}, [&] { dma(0); }, [&] { dma(1); });
    else { dma(0); dma(1); }
    load_frags(f[1], cur, 1);
    mma(f[0], IntC<0>{}, [&] { dma(2); }, [&] { dma(3); });
    load_frags(f[0], cur, 2);
    mma(f[1], IntC<1>{}, [&] { dma(4); }, [&] { dma(5); });
    load_frags(f[1], cur, 3);
    mma(f[0], IntC<2>{}, [&] { dma(6); }, [] {});
    cur = (cur == 2 * STG) ? 0 : cur + STG;
  };
  iter(0, IntC<0>{});
#pragma unroll 1
  for (int s = 1; s < 6; ++s) iter(s, IntC<1>{});
  iter(6, IntC<2>{});
  iter(7, IntC<3>{});
  mma(f[1], IntC<3>{}, [] {}, [] {});
  STAMP(3);
  LAS float* col = (LAS float*)(lds + L_COL);
#pragma unroll
  for (int i = 0; i < (LO ? 3 : 2); ++i) {
    const float v = add_x32(st.cs[i][0] + st.cs[i][1]);
    col[(LO ? w * 3 + i : 12 + (w - 4) * 2 + i) * 32 + r] = v;
  }
  WAITL0();
  BAR();
}

__global__ __launch_bounds__(512, 2) void fused_block(const char* x, const char* wqkv, const char* wphi,
                                                      const char* wo, const char* wgate, const char* wup,
                                                      const char* wdown, char* out, char* ws) {
  extern __shared__ __attribute__((aligned(16))) char smem[];
  LAS char* lds = (LAS char*)smem;
  DIAG_BEGIN();
  asm volatile("" : "+s"(x), "+s"(wqkv), "+s"(wphi), "+s"(wo), "+s"(wgate), "+s"(wup), "+s"(wdown), "+s"(out), "+s"(ws));
  const int lane = threadIdx.x & 63, w = __builtin_amdgcn_readfirstlane(threadIdx.x >> 6);
  const int xcd = blockIdx.x & 7, idx = blockIdx.x >> 3;
  const int ti = 8 * (xcd & 1) + (idx & 7), c = 4 * (xcd >> 1) + (idx >> 3);
  const int t2 = 4 * xcd + (idx & 3), cj = idx >> 2;
  const int th = w & 1, cg = w >> 1, r = lane & 31, h = lane >> 5;
  if (w == 0) glds16(wphi + c * 1024, lane * 16, (unsigned)(size_t)lds + L_WPH);
  if (w == 1 && lane < 32) ((LAS unsigned*)(lds + L_CNT))[lane] = 0u;
  const int f0 = min(96 * c, FF - 96);
  const char* xg = x + (size_t)ti * 128 * 1024;
  const unsigned wl = (lane >> 3) * 1024 + cj * 128 + (((lane & 7) ^ (((lane >> 4) & 1) << 2)) << 4);
  const __amdgpu_buffer_rsrc_t rs = __builtin_amdgcn_make_buffer_rsrc((void*)ws, 0, (int)WS_END, 0x00020000);
  const char *warm1, *warm2;
  {
    const int e = idx * 8 + w, L = min(e, 215) * 64 + lane, sg = 2 + L / 2304, m = L % 2304;
    const int m2 = max(m - 1024, 0), row = 64 * sg + m2 / 20, j = m2 % 20, cb = xcd >> 1;
    const int jj = j < 7 ? j : j - 7;
    const char* pw = j < 14 ? (j < 7 ? wgate : wup) + row * (2 * FF) + min(768 * cb + 128 * jj, 2 * FF - 4)
                            : wqkv + row * 3072 + ((j - 14) >> 1) * 1024 + 256 * cb + 128 * (j & 1);
    warm1 = m < 1024 ? x + (size_t)(1024 * (xcd & 1) + m) * 1024 + sg * 128 : pw;
    const int e2 = min(e, 235);
    warm2 = (e2 < 172 ? wdown + e2 * 8192 : wo + (e2 - 172) * 8192) + lane * 128;
  }
  P1State st;
  if (w < 4) p1_mainloop<true>(st, lds, xg, wgate + 2 * f0, wup + 2 * f0, wqkv + c * 64, wdown, wo, ws, ti, c, wl, w, lane, warm1, warm2 DGPASS);
  else p1_mainloop<false>(st, lds, xg, wgate + 2 * f0, wup + 2 * f0, wqkv + c * 64, wdown, wo, ws, ti, c, wl, w, lane, warm1, warm2 DGPASS);

  const unsigned tag = st.tag;
  const u64 tag64 = ((u64)tag << 32) | (u64)(~tag);
  const int u0 = w < 4 ? cg : 0;
  const float(&rstd)[2] = st.rstd;
  const bf16x8(&mf)[2] = st.mf;
  auto csfrag = [&](float cs) {
    const float ch = bf_hi(cs), cl = cs - ch;
    return __builtin_bit_cast(bf16x8, u32x4{pk2(ch, ch), pk2(cl, 0.f), 0u, 0u});
  };
  {
    LAS float* col = (LAS float*)(lds + L_COL);
    const int b0 = w < 4 ? (w & 2) * 3 : 12 + (w & 2) * 2, bs = w < 4 ? 3 : 2;
    const bf16x8 c0 = csfrag(col[b0 * 32 + r] + col[(b0 + bs) * 32 + r]);
    const bf16x8 c1 = csfrag(col[(b0 + 1) * 32 + r] + col[(b0 + bs + 1) * 32 + r]);
    st.acc[0] = MFMA(c0, mf[0], st.acc[0]);
    st.acc[2] = MFMA(c0, mf[1], st.acc[2]);
    st.acc[1] = MFMA(c1, mf[0], st.acc[1]);
    st.acc[3] = MFMA(c1, mf[1], st.acc[3]);
    if (w < 4) st.acc[4] = MFMA(csfrag((col[2 * 32 + r] + col[5 * 32 + r]) + (col[8 * 32 + r] + col[11 * 32 + r])), mf[0], st.acc[4]);
  }
  STAMP(4);
  auto wfrag = [&](int hh) {
    LAS char* p = lds + L_WPH + hh * 512 + (4 * h + ((lane & 15) >> 2)) * 32 + (lane & 3) * 8;
    s16x4 lo = __builtin_amdgcn_ds_read_tr16_b64_v4i16((LAS s16x4*)p);
    s16x4 hi = __builtin_amdgcn_ds_read_tr16_b64_v4i16((LAS s16x4*)(p + 256));
    const unsigned neg = (lane & 16) ? 0x80008000u : 0u;
    u32x2 a = __builtin_bit_cast(u32x2, lo), b = __builtin_bit_cast(u32x2, hi);
    return __builtin_bit_cast(bf16x8, u32x4{a[0] ^ neg, a[1] ^ neg, b[0] ^ neg, b[1] ^ neg});
  };
  f32x16 zero;
#pragma unroll
  for (int j = 0; j < 16; ++j) zero[j] = 0.f;
  const int jlo = c < 14 ? 0 : (c == 14 ? 4 : 6);
  auto put_h = [&](const u32x4& o, int u, int j) {
    if (j >= jlo) __builtin_amdgcn_raw_buffer_store_b128(o, rs, WS_H + ((4 * ti + 2 * th + (u ^ u0)) * 86 + (f0 >> 4) + j) * 1024 + lane * 16, 0, SC1);
  };
  const int tg = th, kq = cg;
  const bool isS = kq == 3;
  const int st0 = kq == 0 ? 0 : (kq == 1 ? 29 : 57);
  const char* wsrc = (isS ? wo + 19 * 16384 : wdown + (size_t)(st0 + 16) * 16384) + tg * 8192;
  u32x4 wr[13];
  const size_t xoff = ((size_t)t2 * 64 + (threadIdx.x >> 3)) * 1024 + cj * 128 + (threadIdx.x & 7) * 16;
  u32x4 xr;
  if (w >= 4) {
    __builtin_amdgcn_s_setprio(2);
    const int hh = cg - 2;
    bf16x8 idf[2];
#pragma unroll
    for (int s = 0; s < 2; ++s)
#pragma unroll
      for (int j = 0; j < 8; ++j) idf[s][j] = (16 * s + 8 * (j >> 2) + 4 * h + (j & 3) == r) ? (short)0x3f80 : (short)0;
    f32x16 ones;
#pragma unroll
    for (int j = 0; j < 16; ++j) ones[j] = (r >= 16) ? 1.f : 0.f;
    const bf16x8 wf = wfrag(hh);
    f32x16 p0 = MFMA(wf, pack_regs_scaled<0>(st.acc[0], rstd[0]), zero);
    f32x16 p1 = MFMA(wf, pack_regs_scaled<0>(st.acc[2], rstd[1]), zero);
    const f32x16 vt0 = MFMA(pack_regs_scaled<8>(st.acc[0], rstd[0]), idf[0], ones);
    const f32x16 vt1 = MFMA(pack_regs_scaled<8>(st.acc[2], rstd[1]), idf[0], ones);
    dual_softmax(p0);
    f32x16 pt0 = MFMA(pack_regs<0>(p0), idf[0], zero);
    pt0 = MFMA(pack_regs<8>(p0), idf[1], pt0);
    dual_softmax(p1);
    f32x16 pt1 = MFMA(pack_regs<0>(p1), idf[0], zero);
    pt1 = MFMA(pack_regs<8>(p1), idf[1], pt1);
    f32x16 kv = MFMA(pack_regs<0>(pt0), pack_regs<0>(vt0), zero);
    kv = MFMA(pack_regs<8>(pt0), pack_regs<8>(vt0), kv);
    kv = MFMA(pack_regs<0>(pt1), pack_regs<0>(vt1), kv);
    kv = MFMA(pack_regs<8>(pt1), pack_regs<8>(vt1), kv);
    LAS f32x4* ex = (LAS f32x4*)(lds + L_SCR);
    f32x4 give[2], keep[2];
#pragma unroll
    for (int q = 0; q < 2; ++q)
#pragma unroll
      for (int e = 0; e < 4; ++e) { give[q][e] = th ? kv[4 * q + e] : kv[8 + 4 * q + e]; keep[q][e] = th ? kv[8 + 4 * q + e] : kv[4 * q + e]; }
    ex[((w - 4) * 2) * 64 + lane] = give[0];
    ex[((w - 4) * 2 + 1) * 64 + lane] = give[1];
    WAITL0();
    lds_arrive(lds, C_EX + (w - 4), lane);
    lds_wait_ge(lds, C_EX + ((w - 4) ^ 1), 1);
    const unsigned po = WS_KVP + ((c * 16 + ti) * 2 + hh) * 2176 + (r < 16 ? h * 16 + r : 32 + h) * 64 + th * 32;
    if (r <= 16) {
#pragma unroll
      for (int q = 0; q < 2; ++q)
        __builtin_amdgcn_raw_buffer_store_b128(__builtin_bit_cast(u32x4, keep[q] + ex[(((w - 4) ^ 1) * 2 + q) * 64 + lane]), rs, po + q * 16, 0, SC1);
    }
    SB();
    STAMP(5);
    u32x4 h0 = geglu8(st.acc[1], rstd[0]), h1 = geglu8(st.acc[3], rstd[1]);
    asm volatile("" : "+v"(h0), "+v"(h1));
    WAITV(0);
    publish(lds, C_KV, 4, ws, WS_FKV + (c * 16 + ti) * FS, tag64, lane);
    put_h(h0, 0, cg + 2);
    put_h(h1, 1, cg + 2);
    __builtin_amdgcn_s_setprio(0);
    if (!isS) {
      WAITV(0);
      publish(lds, C_H, 8, ws, WS_FH + (ti * 16 + c) * FS, tag64, lane);
      STAMP(6);
    } else {
      {
        const u64 v = __hip_atomic_load((GAS u64*)(ws + WS_FKV + (c * 16 + (lane & 15)) * FS), RLX_AGENT);
        const bool ok = __all(v == tag64);
        WAITV(0);
        publish(lds, C_H, 8, ws, WS_FH + (ti * 16 + c) * FS, tag64, lane);
        STAMP(6);
        if (!ok) poll16(ws, WS_FKV + c * 16 * FS, 0, 15, tag64, lane);
        __builtin_amdgcn_fence(__ATOMIC_ACQUIRE, "wavefront");
      }
      STAMP(7);
      const unsigned go = WS_KVP + (c * 32 + th) * 2176 + lane * 16;
      u32x4 pv[16][3];
#pragma unroll
      for (int t = 0; t < 16; ++t)
#pragma unroll
        for (int q = 0; q < 2; ++q) pv[t][q] = __builtin_amdgcn_raw_buffer_load_b128(rs, go + t * 4352 + q * 1024, 0, SC1);
      const unsigned gz = WS_KVP + (c * 32 + th) * 2176 + 2048 + (lane & 7) * 16;
#pragma unroll
      for (int t = 0; t < 16; ++t) pv[t][2] = __builtin_amdgcn_raw_buffer_load_b128(rs, gz + t * 4352, 0, SC1);
      SB();
      LAS char* kvf = lds + L_KVF + th * 2048;
#pragma unroll
      for (int q = 0; q < 3; ++q) {
        f32x4 a0 = __builtin_bit_cast(f32x4, pv[0][q]);
#pragma unroll
        for (int t = 1; t < 16; ++t) a0 += __builtin_bit_cast(f32x4, pv[t][q]);
        const int fl = q < 2 ? q * 32 + (lane >> 2) : ((lane >> 2) & 1) * 32 + 16;
        *(LAS u32x2*)(kvf + ((lane >> 1) & 1) * 1024 + fl * 16 + (lane & 1) * 8) = u32x2{pk2(a0[0], a0[1]), pk2(a0[2], a0[3])};
      }
      WAITL0();
      const bf16x8 k0 = lds_rd128(kvf + (h * 32 + min(r, 16)) * 16), k1 = lds_rd128(kvf + 1024 + (h * 32 + min(r, 16)) * 16);
      lds_wait_ge(lds, C_PQ, 4);
#pragma unroll
      for (int t = 0; t < 4; ++t) {
        LAS char* pq = lds + L_SCR + (t * 2 + th) * 2048 + lane * 16;
        f32x16 nm = MFMA(k0, lds_rd128(pq), zero);
        nm = MFMA(k1, lds_rd128(pq + 1024), nm);
        const float rd = __builtin_amdgcn_rcpf(nm[8] + 1e-6f);
        __builtin_amdgcn_raw_buffer_store_b128(__builtin_bit_cast(u32x4, pack_regs_scaled<0>(nm, rd)), rs,
                                               WS_O + ((4 * ti + t) * 32 + 2 * c + th) * 1024 + lane * 16, 0, SC1);
      }
      WAITV(0);
      publish(lds, C_O, 2, ws, WS_FO + (ti * 16 + c) * FS, tag64, lane);
      STAMP(8);
    }
  } else {
    auto w2c = [&](int k) {
      const int G = 26 + (w >> 1) + 2 * k;
      glds16(slot_src(G, wdown, wo) + th * 8192, wl, (unsigned)(size_t)lds + slot_lds(G) + th * 1024);
    };
    w2c(0); w2c(1); w2c(2); w2c(3);
    put_h(geglu8(st.acc[0], rstd[0]), 0, 2 * cg);
    SB();
    w2c(4); w2c(5); w2c(6); w2c(7);
    put_h(geglu8(st.acc[1], rstd[0]), 0, 2 * cg + 1);
    put_h(geglu8(st.acc[2], rstd[1]), 1, 2 * cg);
    put_h(geglu8(st.acc[3], rstd[1]), 1, 2 * cg + 1);
    SB();
    STAMP(5);
    f32x16 p0 = MFMA(wfrag(0), pack_regs_scaled<0>(st.acc[4], rstd[0]), zero);
    f32x16 p1 = MFMA(wfrag(1), pack_regs_scaled<8>(st.acc[4], rstd[0]), zero);
    dual_softmax(p0);
    dual_softmax(p1);
    SB();
    WAITV(0);
    publish(lds, C_H, 8, ws, WS_FH + (ti * 16 + c) * FS, tag64, lane);
    STAMP(6);
    lds_wait_ge(lds, C_KV, 4);
    LAS bf16x8* pq = (LAS bf16x8*)(lds + L_SCR + (2 * th + u0) * 4096 + lane * 16);
    pq[0] = pack_regs<0>(p0); pq[64] = pack_regs<8>(p0); pq[128] = pack_regs<0>(p1); pq[192] = pack_regs<8>(p1);
    WAITL0();
    lds_arrive(lds, C_PQ, lane);
    STAMP(8);
  }

  {
#pragma unroll
    for (int i = 0; i < 13; ++i) wr[i] = *(const GAS u32x4*)(wsrc + i * 16384 + wl);
    xr = *(const GAS u32x4*)(x + xoff);
    SB();
    if (isS) poll16(ws, WS_FO + (t2 >> 1) * 16 * FS, 0, 15, tag64, lane);
    else poll16(ws, WS_FH + (t2 >> 1) * 16 * FS, kq == 0 ? 0 : (kq == 1 ? 4 : 9), kq == 0 ? 4 : (kq == 1 ? 9 : 14), tag64, lane);
    STAMP(9);
    if (isS && blockIdx.x == 0 && threadIdx.x == 448) *(GAS unsigned*)ws = tag;
    const unsigned ao = (isS ? WS_O + (2 * t2 + tg) * 32 * 1024 : WS_H + ((2 * t2 + tg) * 86 + st0) * 1024) + lane * 16;
    u32x4 a[32];
    const unsigned ao2 = ao + (isS ? 3072 : 0);
#pragma unroll
    for (int i = 0; i < 16; ++i) a[i] = __builtin_amdgcn_raw_buffer_load_b128(rs, ao + i * 1024, 0, SC1);
    if (isS) {
#pragma unroll
      for (int i = 29; i < 32; ++i) a[i] = __builtin_amdgcn_raw_buffer_load_b128(rs, ao + (i - 13) * 1024, 0, SC1);
    }
#pragma unroll
    for (int i = 16; i < 29; ++i) a[i] = __builtin_amdgcn_raw_buffer_load_b128(rs, ao2 + i * 1024, 0, SC1);
    SB();
    const int q = (lane & 15) >> 2, p = lane & 3, g = (lane >> 4) & 1;
    LAS char* img = lds + slot_lds(16 * kq);
    LAS char* tr[2];
#pragma unroll
    for (int cc = 0; cc < 2; ++cc) tr[cc] = img + (4 * h + q) * 128 + (((4 * cc + 2 * g + (p >> 1)) ^ ((q >> 1) << 2)) << 4) + (p & 1) * 8;
    f32x16 acc[2] = {zero, zero};
    lds_wait_ge(lds, C_H, 8);
    if (kq == 2) a[0] = u32x4{0u, 0u, 0u, 0u};
    auto step = [&](int i, int slot) {
      const bf16x8 w0 = lds_tr2(tr[0] + slot * 2048, tr[0] + slot * 2048 + 1024);
      const bf16x8 w1 = lds_tr2(tr[1] + slot * 2048, tr[1] + slot * 2048 + 1024);
      acc[0] = MFMA(w0, __builtin_bit_cast(bf16x8, a[i]), acc[0]);
      acc[1] = MFMA(w1, __builtin_bit_cast(bf16x8, a[i]), acc[1]);
    };
#pragma unroll
    for (int i = 0; i < 13; ++i) step(i, i);
    WAITL0();
    lds_arrive(lds, C_R1 + w, lane);
    lds_wait_ge(lds, C_R1 + (w ^ 1), 1);
#pragma unroll
    for (int k = 0; k < 13; ++k) *(LAS u32x4*)(img + k * 2048 + tg * 1024 + lane * 16) = wr[k];
    WAITL0();
    lds_arrive(lds, C_R2 + w, lane);
#pragma unroll
    for (int i = 13; i < 16; ++i) step(i, i);
    if (isS) { step(29, 16); step(30, 17); step(31, 18); }
    lds_wait_ge(lds, C_R2 + (w ^ 1), 1);
    STAMP(10);
#pragma unroll
    for (int i = 16; i < 29; ++i) step(i, i - 16);
    STAMP(11);
    __syncthreads();
    {
      LAS char* row = lds + (kq * 64 + tg * 32 + r) * 272 + h * 16;
#pragma unroll
      for (int cc = 0; cc < 2; ++cc)
#pragma unroll
        for (int k = 0; k < 4; ++k)
          *(LAS f32x4*)(row + cc * 128 + k * 32) = f32x4{acc[cc][4 * k], acc[cc][4 * k + 1], acc[cc][4 * k + 2], acc[cc][4 * k + 3]};
    }
    __syncthreads();
    {
      LAS char* rp = lds + (threadIdx.x >> 3) * 272 + (threadIdx.x & 7) * 32;
      float res[8];
#pragma unroll
      for (int hf = 0; hf < 2; ++hf) {
        const f32x4 gg = *(LAS f32x4*)(rp + hf * 16) + *(LAS f32x4*)(rp + 64 * 272 + hf * 16) + *(LAS f32x4*)(rp + 128 * 272 + hf * 16);
        const f32x4 ss = *(LAS f32x4*)(rp + 192 * 272 + hf * 16);
#pragma unroll
        for (int e = 0; e < 4; ++e) {
          const unsigned xw = xr[hf * 2 + (e >> 1)];
          const float xv = asf((e & 1) ? (xw & 0xffff0000u) : (xw << 16));
          const float sg = gg[e] * __builtin_amdgcn_rcpf(1.f + __builtin_amdgcn_exp2f(-gg[e] * LOG2E));
          res[hf * 4 + e] = __builtin_fmaf(ss[e], sg, xv);
        }
      }
      *(GAS bf16x8*)(out + xoff) = pack8(res[0], res[1], res[2], res[3], res[4], res[5], res[6], res[7]);
    }
    STAMP(12);
  }
  DIAG_END();
}

extern "C" void kernel_launch(void* const* d_in, const int* in_sizes, int n_in, void* d_out, int out_size, void* d_ws, size_t ws_size,
                              hipStream_t stream) {
  (void)hipFuncSetAttribute((const void*)fused_block, hipFuncAttributeMaxDynamicSharedMemorySize, LDS_BYTES);
  fused_block<<<256, 512, LDS_BYTES, stream>>>((const char*)d_in[0], (const char*)d_in[1], (const char*)d_in[2], (const char*)d_in[3],
                                               (const char*)d_in[5], (const char*)d_in[6], (const char*)d_in[7], (char*)d_out, (char*)d_ws);
}
```

```cpp
#include <hip/hip_runtime.h>
#include <hip/hip_bf16.h>

#define LAS __attribute__((address_space(3)))
#define GAS __attribute__((address_space(1)))
#define DI __device__ __forceinline__
typedef short bf16x8 __attribute__((ext_vector_type(8)));
typedef short s16x4 __attribute__((ext_vector_type(4)));
typedef float f32x16 __attribute__((ext_vector_type(16)));
typedef float f32x4 __attribute__((ext_vector_type(4)));
typedef float f32x2 __attribute__((ext_vector_type(2)));
typedef unsigned u32x4 __attribute__((ext_vector_type(4)));
typedef unsigned u32x2 __attribute__((ext_vector_type(2)));
typedef __bf16 bf2 __attribute__((ext_vector_type(2)));
typedef unsigned long long u64;

constexpr int DM = 512, FF = 1376;
constexpr int XB = 16384, RG = 12288, GOFF = XB, UOFF = XB + RG + 32, QOFF = XB + 2 * RG + 32, STG = XB + 3 * RG + 32;
constexpr int L_WPH = 3 * STG, L_CNT = L_WPH + 1024, L_COL = L_CNT + 128;
constexpr int L_SCR = 65536, SCR_BYTES = 20992, L_KVF = L_SCR + 16384;
constexpr int LDS_BYTES = 163840;
constexpr int L_DUMMY = L_COL + 2560;
static_assert(L_DUMMY + 256 <= LDS_BYTES, "lds");
enum { C_KV = 0, C_H = 1, C_O = 2, C_PQ = 3, C_EX = 4  , C_R1 = 8  , C_R2 = 16   };
constexpr unsigned FS = 256;
constexpr unsigned WS_ST = 4096, WS_FKV = 65536, WS_FH = 2 * 65536, WS_FO = 3 * 65536;
constexpr unsigned WS_H = 4 * 65536;
constexpr unsigned WS_O = WS_H + 64u * 86 * 1024;
constexpr unsigned WS_KVP = WS_O + 64u * 32 * 1024;
constexpr unsigned WS_END = WS_KVP + 512u * 2176;
#define PSLEEP 2
constexpr int SC1 = 16;
#define NO_WARM2
#define WARM2_AT 2
constexpr unsigned SPIN_G = 1u << 15, SPIN_L = 1u << 19;

#define STAMP(i) do {} while (0)
#define DIAG_BEGIN() do {} while (0)
#define DIAG_END() do {} while (0)
#define DGARG
#define DGPASS

#define XW12 "13"
#define XW14 "15"
#define MFMA(a, b, c) __builtin_amdgcn_mfma_f32_32x32x16_bf16((a), (b), (c), 0, 0, 0)
#define WAITV(n) asm volatile("s_waitcnt vmcnt(" #n ")" ::: "memory")
#define WAITL0() asm volatile("s_waitcnt lgkmcnt(0)" ::: "memory")
#define BAR() __builtin_amdgcn_s_barrier()
#define SB() __builtin_amdgcn_sched_barrier(0)
#define RLX_AGENT __ATOMIC_RELAXED, __HIP_MEMORY_SCOPE_AGENT
#define RLX_WG __ATOMIC_RELAXED, __HIP_MEMORY_SCOPE_WORKGROUP

DI float asf(unsigned u) { return __builtin_bit_cast(float, u); }
DI unsigned asu(float f) { return __builtin_bit_cast(unsigned, f); }
DI unsigned pk2(float a, float b) {
  f32x2 f = {a, b};
  return __builtin_bit_cast(unsigned, __builtin_convertvector(f, bf2));
}
DI bf16x8 pack8(float a0, float a1, float a2, float a3, float a4, float a5, float a6, float a7) {
  u32x4 p = {pk2(a0, a1), pk2(a2, a3), pk2(a4, a5), pk2(a6, a7)};
  return __builtin_bit_cast(bf16x8, p);
}
template <int S0> DI bf16x8 pack_regs(const f32x16& x) {
  return pack8(x[S0], x[S0 + 1], x[S0 + 2], x[S0 + 3], x[S0 + 4], x[S0 + 5], x[S0 + 6], x[S0 + 7]);
}
template <int S0> DI bf16x8 pack_regs_scaled(const f32x16& x, float s) {
  return pack8(x[S0] * s, x[S0 + 1] * s, x[S0 + 2] * s, x[S0 + 3] * s, x[S0 + 4] * s, x[S0 + 5] * s, x[S0 + 6] * s, x[S0 + 7] * s);
}
DI float bf_hi(float v) { return asf(pk2(0.f, v) & 0xffff0000u); }
DI void glds16(const char* sbase, unsigned voff, unsigned ldst) {
  asm volatile("s_mov_b32 m0, %2\n\ts_nop 0\n\tglobal_load_lds_dwordx4 %0, %1" ::"v"(voff), "s"(sbase), "s"(ldst) : "memory");
}
DI void warm(const char* lane_ptr, unsigned ldst) {
  asm volatile("s_mov_b32 m0, %1\n\ts_nop 0\n\tglobal_load_lds_dword %0, off" ::"v"(lane_ptr), "s"(ldst) : "memory");
}
DI bf16x8 lds_rd128(LAS char* p) { return *(LAS bf16x8*)p; }
DI bf16x8 lds_tr2(LAS char* p0, LAS char* p1) {
  s16x4 lo = __builtin_amdgcn_ds_read_tr16_b64_v4i16((LAS s16x4*)p0);
  s16x4 hi = __builtin_amdgcn_ds_read_tr16_b64_v4i16((LAS s16x4*)p1);
  return __builtin_shufflevector(lo, hi, 0, 1, 2, 3, 4, 5, 6, 7);
}
DI void acc_pair(unsigned u, float& a0, float& a1) {
  unsigned t0, t1;
  asm volatile("v_lshlrev_b32 %2, 16, %4\n\tv_and_b32 %3, %5, %4\n\tv_add_f32 %0, %0, %2\n\tv_add_f32 %1, %1, %3"
               : "+v"(a0), "+v"(a1), "=&v"(t0), "=&v"(t1) : "v"(u), "s"(0xffff0000u));
}
#define MX(a, b) __builtin_elementwise_maximum((a), (b))
DI float max_x32(float v) {
  auto r = __builtin_amdgcn_permlane32_swap(asu(v), asu(v), false, false);
  const unsigned r0 = r[0], r1 = r[1];
  return MX(asf(r0), asf(r1));
}
DI float add_x32(float v) {
  auto r = __builtin_amdgcn_permlane32_swap(asu(v), asu(v), false, false);
  const unsigned r0 = r[0], r1 = r[1];
  return asf(r0) + asf(r1);
}
template <int CTRL> DI float dpp_add(float v) { return v + asf(__builtin_amdgcn_update_dpp(0u, asu(v), CTRL, 0xf, 0xf, true)); }
DI float wave_sum(float v) {
  v = dpp_add<0xB1>(v); v = dpp_add<0x4E>(v); v = dpp_add<0x141>(v); v = dpp_add<0x140>(v);
  auto r = __builtin_amdgcn_permlane16_swap(asu(v), asu(v), false, false);
  const unsigned r0 = r[0], r1 = r[1];
  return add_x32(asf(r0) + asf(r1));
}
constexpr float LOG2E = 1.4426950408889634f;

DI void dual_softmax(f32x16& p) {
  float m0 = MX(MX(MX(MX(p[0], p[1]), p[2]), MX(MX(p[3], p[4]), p[5])), MX(p[6], p[7]));
  float m1 = MX(MX(MX(MX(p[8], p[9]), p[10]), MX(MX(p[11], p[12]), p[13])), MX(p[14], p[15]));
  m0 = max_x32(m0) * -LOG2E;
  m1 = max_x32(m1) * -LOG2E;
#pragma unroll
  for (int i = 0; i < 8; ++i) {
    p[i] = __builtin_amdgcn_exp2f(__builtin_fmaf(p[i], LOG2E, m0));
    p[8 + i] = __builtin_amdgcn_exp2f(__builtin_fmaf(p[8 + i], LOG2E, m1));
  }
  const float s0 = add_x32(((p[0] + p[1]) + (p[2] + p[3])) + ((p[4] + p[5]) + (p[6] + p[7])));
  const float s1 = add_x32(((p[8] + p[9]) + (p[10] + p[11])) + ((p[12] + p[13]) + (p[14] + p[15])));
  const float r0 = __builtin_amdgcn_rcpf(s0), r1 = __builtin_amdgcn_rcpf(s1);
#pragma unroll
  for (int i = 0; i < 8; ++i) { p[i] *= r0; p[8 + i] *= r1; }
}
DI u32x4 geglu8(const f32x16& acc, float rs) {
  const float c1 = -2.f * 0.7978845608028654f * LOG2E, c2 = c1 * 0.044715f;
  const float k1 = c1 * rs, rs2 = rs * rs, k2 = c2 * rs2 * rs, ir2 = __builtin_amdgcn_rcpf(rs2);
  float hv[8];
#pragma unroll
  for (int e = 0; e < 8; ++e) {
    const float a = acc[e];
    const float ex = __builtin_amdgcn_exp2f(a * __builtin_fmaf(a * a, k2, k1));
    hv[e] = (a * acc[e + 8]) * __builtin_amdgcn_rcpf(__builtin_fmaf(ex, ir2, ir2));
  }
  return u32x4{pk2(hv[0], hv[1]), pk2(hv[2], hv[3]), pk2(hv[4], hv[5]), pk2(hv[6], hv[7])};
}
DI void lds_wait_ge(LAS char* lds, int idx, unsigned want) {
  volatile LAS unsigned* p = (volatile LAS unsigned*)(lds + L_CNT) + idx;
#pragma unroll 1
  for (unsigned s = 0; s < SPIN_L; ++s) {
    if (__builtin_amdgcn_readfirstlane(*p) >= want) break;
    __builtin_amdgcn_s_sleep(1);
  }
  asm volatile("" ::: "memory");
}
DI unsigned lds_arrive(LAS char* lds, int idx, int lane) {
  unsigned old = 0;
  if (lane == 0) old = __hip_atomic_fetch_add((LAS unsigned*)(lds + L_CNT) + idx, 1u, RLX_WG);
  return __builtin_amdgcn_readfirstlane(old);
}
DI void publish(LAS char* lds, int idx, unsigned n, char* ws, unsigned flag_off, u64 tag64, int lane) {
  if (lds_arrive(lds, idx, lane) == n - 1 && lane == 0) __hip_atomic_store((GAS u64*)(ws + flag_off), tag64, RLX_AGENT);
}
DI void poll16(const char* ws, unsigned off, int lo, int hi, u64 tag64, int lane) {
  const int l = min(max(lane & 15, lo), hi);
  GAS u64* p = (GAS u64*)(ws + off + l * FS);
#pragma unroll 1
  for (unsigned s = 0; s < SPIN_G; ++s) {
    const u64 v = __hip_atomic_load(p, RLX_AGENT);
    if (__all(v == tag64)) break;
    __builtin_amdgcn_s_sleep(PSLEEP);
  }
  __builtin_amdgcn_fence(__ATOMIC_ACQUIRE, "wavefront");
}

template <bool V> struct BoolC { static constexpr bool value = V; };
template <int V> struct IntC { static constexpr int value = V; };
struct Frags { bf16x8 x[2]; bf16x8 w[3]; };
struct P1State { f32x16 acc[5]; float cs[3][2]; float rstd[2]; bf16x8 mf[2]; unsigned tag; };

DI unsigned slot_lds(int G) { return 2048u * G + (G >= 32 ? (unsigned)SCR_BYTES : 0u); }
DI const char* slot_src(int G, const char* wdown, const char* wo) {
  return G < 48 ? wdown + (size_t)(G + (G >= 16 ? 13 : 0) + (G >= 32 ? 12 : 0)) * 16384 : wo + (size_t)(G - 48) * 16384;
}

template <bool LO>
DI void p1_mainloop(P1State& st, LAS char* lds, const char* xg, const char* wG, const char* wU, const char* wQ, const char* wdown, const char* wo,
                    char* ws, int ti, int c, unsigned wl, int w, int lane, const char* warm1, const char* warm2 DGARG) {
  const int th = w & 1, cg = w >> 1, r = lane & 31, h = lane >> 5;
  const unsigned lb = (unsigned)(size_t)lds;
  u32x4 xrow;
  const int myrow = 8 * c + w;
  asm volatile("global_load_dwordx4 %0, %1, %2" : "=v"(xrow) : "v"(myrow * 1024 + lane * 16), "s"(xg) : "memory");
  unsigned xo[2], go[2], qo[2];
#pragma unroll
  for (int a = 0; a < 2; ++a) {
    const int row = 8 * (2 * w + a) + (lane >> 3);
    xo[a] = row * 1024 + (((lane & 7) ^ ((row >> 1) & 7)) << 4);
    const int p = a == 0 ? w : 8 + (w & 3);
    const int q = 64 * p + lane, rr = q / 12, cc = q - rr * 12;
    go[a] = rr * (2 * FF) + cc * 16;
    const int mat = cc < 8 ? 1 + ((cc >> 1) & 1) : 0, hh = cc < 8 ? cc >> 2 : (cc - 8) >> 1;
    qo[a] = rr * 3072 + mat * 1024 + hh * 32 + (cc & 1) * 16;
  }
  const int p2 = 8 + (w & 3);
  auto piece = [&](int k, int s, unsigned bo) {
    const unsigned b = lb + bo;
    if (k == 0) glds16(xg + s * 128, xo[0], b + 2 * w * 1024);
    if (k == 1) glds16(wG + s * (128 * FF), go[0], b + GOFF + w * 1024);
    if (k == 2) glds16(wU + s * (128 * FF), go[0], b + UOFF + w * 1024);
    if (k == 3) glds16(wQ + s * (64 * 3072), qo[0], b + QOFF + w * 1024);
    if (k == 4) glds16(xg + s * 128, xo[1], b + (2 * w + 1) * 1024);
    if (k == 5) glds16((LO ? wG : wU) + s * (128 * FF), go[1], b + (LO ? GOFF : UOFF) + p2 * 1024);
    if (k == 6 && !LO) glds16(wQ + s * (64 * 3072), qo[1], b + QOFF + p2 * 1024);
  };
  auto w2piece = [&](int k, int G0, int nslot) {
    const int L = (w >> 1) + 4 * k;
    if (4 * k + 3 < nslot || L < nslot) glds16(slot_src(G0 + L, wdown, wo) + th * 8192, wl, lb + slot_lds(G0 + L) + th * 1024);
  };
#pragma unroll
  for (int k = 0; k < 7; ++k) piece(k, 0, 0);
  BAR();
#pragma unroll
  for (int k = 0; k < 7; ++k) piece(k, 1, STG);
  warm(warm1, lb + L_DUMMY);
  unsigned tag;
  asm volatile("s_load_dword %0, %1, 0x0" : "=s"(tag) : "s"(ws) : "memory");
  const int rot = LO ? 2 * th + cg : 2 * th;
  const int g = (lane >> 4) & 1, u0 = LO ? cg : 0;
  const int wrow = (8 * h + ((lane & 15) >> 2)) * 192 + (lane & 3) * 8;
  const int wFF = (g ? UOFF : GOFF) + wrow + (LO ? 64 * cg : 32 * (cg + 2));
  const int wQK = QOFF + wrow + g * 32 + (LO ? 128 : 64 * (cg - 2));
  int xad[4], kof[4];
#pragma unroll
  for (int kk = 0; kk < 4; ++kk) {
    const int ks = (kk + rot) & 3;
    xad[kk] = (64 * th + r) * 128 + (((2 * ks + h) ^ ((r >> 1) & 7)) << 4);
    kof[kk] = ks * 3072;
  }
  const int xa0 = u0 * 4096, xa1 = 4096 - xa0;
  auto load_frags = [&](Frags& f, int bo, int kk) {
    LAS char* xa = lds + bo + xad[kk];
    LAS char* a = lds + (bo + kof[kk]) + wFF;
    LAS char* q = lds + (bo + kof[kk]) + wQK;
    f.x[0] = lds_rd128(xa + xa0);
    if (LO) { f.w[0] = lds_tr2(a, a + 768); f.w[1] = lds_tr2(a + 32, a + 32 + 768); }
    else { f.w[0] = lds_tr2(q, q + 768); f.w[1] = lds_tr2(a, a + 768); }
    f.x[1] = lds_rd128(xa + xa1);
    if (LO) f.w[2] = lds_tr2(q, q + 768);
  };
#pragma unroll
  for (int i = 0; i < 5; ++i)
#pragma unroll
    for (int j = 0; j < 16; ++j) st.acc[i][j] = 0.f;
#pragma unroll
  for (int i = 0; i < 3; ++i) st.cs[i][0] = st.cs[i][1] = 0.f;
  if (LO) asm volatile("s_waitcnt vmcnt(" XW12 ") lgkmcnt(0)" : "+v"(xrow), "+s"(tag)::"memory");
  else asm volatile("s_waitcnt vmcnt(" XW14 ") lgkmcnt(0)" : "+v"(xrow), "+s"(tag)::"memory");
  tag += 1u;
  st.tag = tag;
  {
    float v[8];
#pragma unroll
    for (int i = 0; i < 4; ++i) { const unsigned t = xrow[i]; v[2 * i] = asf(t << 16); v[2 * i + 1] = asf(t & 0xffff0000u); }
    const float mean = wave_sum(((v[0] + v[1]) + (v[2] + v[3])) + ((v[4] + v[5]) + (v[6] + v[7]))) * (1.f / DM);
    float ss = 0.f;
#pragma unroll
    for (int i = 0; i < 8; ++i) { const float d = v[i] - mean; ss = __builtin_fmaf(d, d, ss); }
    const float rstd = __builtin_amdgcn_rsqf(wave_sum(ss) * (1.f / DM) + 1e-5f);
    const u32x4 gr = {asu(mean), tag, asu(rstd), tag};
    if (lane == 0)
      asm volatile("global_store_dwordx4 %0, %1, %2 sc1\n\ts_nop 1" ::"v"(WS_ST + (ti * 128 + myrow) * 16), "v"(gr), "s"(ws) : "memory");
  }
  STAMP(1);
  Frags f[2];
  u32x4 stt[2];
  auto mma = [&](const Frags& cf, auto kk_tag, auto&& dma0, auto&& dma1) {
    constexpr int kk = decltype(kk_tag)::value;
    const u32x4 cw = __builtin_bit_cast(u32x4, cf.w[kk < 2 ? 0 : 1]);
    float(&cs)[2] = st.cs[kk < 2 ? 0 : 1];
    SB();
    st.acc[0] = MFMA(cf.w[0], cf.x[0], st.acc[0]); acc_pair(cw[0], cs[0], cs[1]); SB();
    st.acc[1] = MFMA(cf.w[1], cf.x[0], st.acc[1]); acc_pair(cw[1], cs[0], cs[1]); dma0(); SB();
    st.acc[2] = MFMA(cf.w[0], cf.x[1], st.acc[2]); acc_pair(cw[2], cs[0], cs[1]); SB();
    st.acc[3] = MFMA(cf.w[1], cf.x[1], st.acc[3]); acc_pair(cw[3], cs[0], cs[1]); dma1(); SB();
    if (LO) {
      st.acc[4] = MFMA(cf.w[2], cf.x[0], st.acc[4]);
      if (kk == 0) {
        const u32x4 cq = __builtin_bit_cast(u32x4, cf.w[2]);
#pragma unroll
        for (int i = 0; i < 4; ++i) acc_pair(cq[i], st.cs[2][0], st.cs[2][1]);
      }
      SB();
    }
  };
  int cur = 0;
  auto iter = [&](int s, auto mode_tag) {
    constexpr int MODE = decltype(mode_tag)::value;
    const int fre = (cur == 0) ? 2 * STG : cur - STG;
    auto dma = [&](int k) {
      if (MODE <= 1) piece(k, s + 2, fre);
      if (MODE == 2) w2piece(k, 42, 25);
      if (MODE == 3) w2piece(k, 0, 26);
    };
    WAITL0();
    if (MODE == 3) asm volatile("s_waitcnt vmcnt(6)" : "+v"(stt[0]), "+v"(stt[1])::"memory"); else WAITV(6);
    BAR();
    if (MODE == 0) STAMP(2);
    load_frags(f[0], cur, 0);
    if (MODE == 3) {
#pragma unroll 1
      for (unsigned sp = 0; sp < SPIN_G; ++sp) {
        if (__all((((stt[0][1] ^ tag) | (stt[0][3] ^ tag)) | ((stt[1][1] ^ tag) | (stt[1][3] ^ tag))) == 0u)) break;
        asm volatile("global_load_dwordx4 %0, %2, %4 sc1\n\tglobal_load_dwordx4 %1, %3, %4 sc1\n\ts_waitcnt vmcnt(0)"
                     : "=&v"(stt[0]), "=&v"(stt[1])
                     : "v"(WS_ST + (ti * 128 + 64 * th + 32 * u0 + r) * 16), "v"(WS_ST + (ti * 128 + 64 * th + 32 * (1 ^ u0) + r) * 16), "s"(ws)
                     : "memory");
      }
#pragma unroll
      for (int u = 0; u < 2; ++u) {
        const unsigned mu = stt[u][0], ru = stt[u][2];
        const float mean = h ? 0.f : -asf(mu);
        st.rstd[u] = asf(ru);
        const float mh = bf_hi(mean);
        st.mf[u] = __builtin_bit_cast(bf16x8, u32x4{pk2(mh, mean - mh), pk2(mh, 0.f), 0u, 0u});
      }
    }
    if (MODE == 2) {
#pragma unroll
      for (int u = 0; u < 2; ++u)
        asm volatile("global_load_dwordx4 %0, %1, %2 sc1" : "=v"(stt[u]) : "v"(WS_ST + (ti * 128 + 64 * th + 32 * (u ^ u0) + r) * 16), "s"(ws) : "memory");
    }
    if (MODE != 0) mma(f[1], IntC<3>{}, [&] { dma(0); }, [&] { dma(1); });
    else { dma(0); dma(1); }
    load_frags(f[1], cur, 1);
    mma(f[0], IntC<0>{}, [&] { dma(2); }, [&] { dma(3); });
    load_frags(f[0], cur, 2);
    mma(f[1], IntC<1>{}, [&] { dma(4); }, [&] { dma(5); });
    load_frags(f[1], cur, 3);
    mma(f[0], IntC<2>{}, [&] { dma(6); }, [] {});
    cur = (cur == 2 * STG) ? 0 : cur + STG;
  };
  iter(0, IntC<0>{});
#pragma unroll 1
  for (int s = 1; s < 6; ++s) iter(s, IntC<1>{});
  iter(6, IntC<2>{});
  iter(7, IntC<3>{});
  mma(f[1], IntC<3>{}, [] {}, [] {});
  STAMP(3);
  LAS float* col = (LAS float*)(lds + L_COL);
#pragma unroll
  for (int i = 0; i < (LO ? 3 : 2); ++i) {
    const float v = add_x32(st.cs[i][0] + st.cs[i][1]);
    col[(LO ? w * 3 + i : 12 + (w - 4) * 2 + i) * 32 + r] = v;
  }
  WAITL0();
  BAR();
}

__global__ __launch_bounds__(512, 2) void fused_block(const char* x, const char* wqkv, const char* wphi,
                                                      const char* wo, const char* wgate, const char* wup,
                                                      const char* wdown, char* out, char* ws) {
  extern __shared__ __attribute__((aligned(16))) char smem[];
  LAS char* lds = (LAS char*)smem;
  DIAG_BEGIN();
  asm volatile("" : "+s"(x), "+s"(wqkv), "+s"(wphi), "+s"(wo), "+s"(wgate), "+s"(wup), "+s"(wdown), "+s"(out), "+s"(ws));
  const int lane = threadIdx.x & 63, w = __builtin_amdgcn_readfirstlane(threadIdx.x >> 6);
  const int xcd = blockIdx.x & 7, idx = blockIdx.x >> 3;
  const int ti = 8 * (xcd & 1) + (idx & 7), c = 4 * (xcd >> 1) + (idx >> 3);
  const int t2 = 4 * xcd + (idx & 3), cj = idx >> 2;
  const int th = w & 1, cg = w >> 1, r = lane & 31, h = lane >> 5;
  if (w == 0) glds16(wphi + c * 1024, lane * 16, (unsigned)(size_t)lds + L_WPH);
  if (w == 1 && lane < 32) ((LAS unsigned*)(lds + L_CNT))[lane] = 0u;
  const int f0 = min(96 * c, FF - 96);
  const char* xg = x + (size_t)ti * 128 * 1024;
  const unsigned wl = (lane >> 3) * 1024 + cj * 128 + (((lane & 7) ^ (((lane >> 4) & 1) << 2)) << 4);
  const __amdgpu_buffer_rsrc_t rs = __builtin_amdgcn_make_buffer_rsrc((void*)ws, 0, (int)WS_END, 0x00020000);
  const char *warm1, *warm2;
  {
    const int e = idx * 8 + w;
    const int l = lane & 31;
    warm1 = l < 18 ? ws + min((unsigned)l << 19, WS_END - 4) : l < 22 ? out + ((l - 18) << 19) : l < 25 ? wdown + min((l - 22) << 19, 1376 * 1024 - 4) : l < 27 ? wo + (l - 25) * (512 * 1024 - 4) : ws;
    const int e2 = min(e, 235);
    warm2 = (e2 < 172 ? wdown + e2 * 8192 : wo + (e2 - 172) * 8192) + lane * 128;
  }
  P1State st;
  if (w < 4) p1_mainloop<true>(st, lds, xg, wgate + 2 * f0, wup + 2 * f0, wqkv + c * 64, wdown, wo, ws, ti, c, wl, w, lane, warm1, warm2 DGPASS);
  else p1_mainloop<false>(st, lds, xg, wgate + 2 * f0, wup + 2 * f0, wqkv + c * 64, wdown, wo, ws, ti, c, wl, w, lane, warm1, warm2 DGPASS);

  const unsigned tag = st.tag;
  const u64 tag64 = ((u64)tag << 32) | (u64)(~tag);
  const int u0 = w < 4 ? cg : 0;
  const float(&rstd)[2] = st.rstd;
  const bf16x8(&mf)[2] = st.mf;
  auto csfrag = [&](float cs) {
    const float ch = bf_hi(cs), cl = cs - ch;
    return __builtin_bit_cast(bf16x8, u32x4{pk2(ch, ch), pk2(cl, 0.f), 0u, 0u});
  };
  {
    LAS float* col = (LAS float*)(lds + L_COL);
    const int b0 = w < 4 ? (w & 2) * 3 : 12 + (w & 2) * 2, bs = w < 4 ? 3 : 2;
    const bf16x8 c0 = csfrag(col[b0 * 32 + r] + col[(b0 + bs) * 32 + r]);
    const bf16x8 c1 = csfrag(col[(b0 + 1) * 32 + r] + col[(b0 + bs + 1) * 32 + r]);
    st.acc[0] = MFMA(c0, mf[0], st.acc[0]);
    st.acc[2] = MFMA(c0, mf[1], st.acc[2]);
    st.acc[1] = MFMA(c1, mf[0], st.acc[1]);
    st.acc[3] = MFMA(c1, mf[1], st.acc[3]);
    if (w < 4) st.acc[4] = MFMA(csfrag((col[2 * 32 + r] + col[5 * 32 + r]) + (col[8 * 32 + r] + col[11 * 32 + r])), mf[0], st.acc[4]);
  }
  STAMP(4);
  auto wfrag = [&](int hh) {
    LAS char* p = lds + L_WPH + hh * 512 + (4 * h + ((lane & 15) >> 2)) * 32 + (lane & 3) * 8;
    s16x4 lo = __builtin_amdgcn_ds_read_tr16_b64_v4i16((LAS s16x4*)p);
    s16x4 hi = __builtin_amdgcn_ds_read_tr16_b64_v4i16((LAS s16x4*)(p + 256));
    const unsigned neg = (lane & 16) ? 0x80008000u : 0u;
    u32x2 a = __builtin_bit_cast(u32x2, lo), b = __builtin_bit_cast(u32x2, hi);
    return __builtin_bit_cast(bf16x8, u32x4{a[0] ^ neg, a[1] ^ neg, b[0] ^ neg, b[1] ^ neg});
  };
  f32x16 zero;
#pragma unroll
  for (int j = 0; j < 16; ++j) zero[j] = 0.f;
  const int jlo = c < 14 ? 0 : (c == 14 ? 4 : 6);
  auto put_h = [&](const u32x4& o, int u, int j) {
    if (j >= jlo) __builtin_amdgcn_raw_buffer_store_b128(o, rs, WS_H + ((4 * ti + 2 * th + (u ^ u0)) * 86 + (f0 >> 4) + j) * 1024 + lane * 16, 0, SC1);
  };
  const int tg = th, kq = cg;
  const bool isS = kq == 3;
  const int st0 = kq == 0 ? 0 : (kq == 1 ? 29 : 57);
  const char* wsrc = (isS ? wo + 19 * 16384 : wdown + (size_t)(st0 + 16) * 16384) + tg * 8192;
  u32x4 wr[13];
  const size_t xoff = ((size_t)t2 * 64 + (threadIdx.x >> 3)) * 1024 + cj * 128 + (threadIdx.x & 7) * 16;
  u32x4 xr;
  if (w >= 4) {
    __builtin_amdgcn_s_setprio(2);
    const int hh = cg - 2;
    bf16x8 idf[2];
#pragma unroll
    for (int s = 0; s < 2; ++s)
#pragma unroll
      for (int j = 0; j < 8; ++j) idf[s][j] = (16 * s + 8 * (j >> 2) + 4 * h + (j & 3) == r) ? (short)0x3f80 : (short)0;
    f32x16 ones;
#pragma unroll
    for (int j = 0; j < 16; ++j) ones[j] = (r >= 16) ? 1.f : 0.f;
    const bf16x8 wf = wfrag(hh);
    f32x16 p0 = MFMA(wf, pack_regs_scaled<0>(st.acc[0], rstd[0]), zero);
    f32x16 p1 = MFMA(wf, pack_regs_scaled<0>(st.acc[2], rstd[1]), zero);
    const f32x16 vt0 = MFMA(pack_regs_scaled<8>(st.acc[0], rstd[0]), idf[0], ones);
    const f32x16 vt1 = MFMA(pack_regs_scaled<8>(st.acc[2], rstd[1]), idf[0], ones);
    dual_softmax(p0);
    f32x16 pt0 = MFMA(pack_regs<0>(p0), idf[0], zero);
    pt0 = MFMA(pack_regs<8>(p0), idf[1], pt0);
    dual_softmax(p1);
    f32x16 pt1 = MFMA(pack_regs<0>(p1), idf[0], zero);
    pt1 = MFMA(pack_regs<8>(p1), idf[1], pt1);
    f32x16 kv = MFMA(pack_regs<0>(pt0), pack_regs<0>(vt0), zero);
    kv = MFMA(pack_regs<8>(pt0), pack_regs<8>(vt0), kv);
    kv = MFMA(pack_regs<0>(pt1), pack_regs<0>(vt1), kv);
    kv = MFMA(pack_regs<8>(pt1), pack_regs<8>(vt1), kv);
    LAS f32x4* ex = (LAS f32x4*)(lds + L_SCR);
    f32x4 give[2], keep[2];
#pragma unroll
    for (int q = 0; q < 2; ++q)
#pragma unroll
      for (int e = 0; e < 4; ++e) { give[q][e] = th ? kv[4 * q + e] : kv[8 + 4 * q + e]; keep[q][e] = th ? kv[8 + 4 * q + e] : kv[4 * q + e]; }
    ex[((w - 4) * 2) * 64 + lane] = give[0];
    ex[((w - 4) * 2 + 1) * 64 + lane] = give[1];
    WAITL0();
    lds_arrive(lds, C_EX + (w - 4), lane);
    lds_wait_ge(lds, C_EX + ((w - 4) ^ 1), 1);
    const unsigned po = WS_KVP + ((c * 16 + ti) * 2 + hh) * 2176 + (r < 16 ? h * 16 + r : 32 + h) * 64 + th * 32;
    if (r <= 16) {
#pragma unroll
      for (int q = 0; q < 2; ++q)
        __builtin_amdgcn_raw_buffer_store_b128(__builtin_bit_cast(u32x4, keep[q] + ex[(((w - 4) ^ 1) * 2 + q) * 64 + lane]), rs, po + q * 16, 0, SC1);
    }
    SB();
    STAMP(5);
    u32x4 h0 = geglu8(st.acc[1], rstd[0]), h1 = geglu8(st.acc[3], rstd[1]);
    asm volatile("" : "+v"(h0), "+v"(h1));
    WAITV(0);
    publish(lds, C_KV, 4, ws, WS_FKV + (c * 16 + ti) * FS, tag64, lane);
    put_h(h0, 0, cg + 2);
    put_h(h1, 1, cg + 2);
    __builtin_amdgcn_s_setprio(0);
    if (!isS) {
      WAITV(0);
      publish(lds, C_H, 8, ws, WS_FH + (ti * 16 + c) * FS, tag64, lane);
      STAMP(6);
    } else {
      {
        const u64 v = __hip_atomic_load((GAS u64*)(ws + WS_FKV + (c * 16 + (lane & 15)) * FS), RLX_AGENT);
        const bool ok = __all(v == tag64);
        WAITV(0);
        publish(lds, C_H, 8, ws, WS_FH + (ti * 16 + c) * FS, tag64, lane);
        STAMP(6);
        if (!ok) poll16(ws, WS_FKV + c * 16 * FS, 0, 15, tag64, lane);
        __builtin_amdgcn_fence(__ATOMIC_ACQUIRE, "wavefront");
      }
      STAMP(7);
      const unsigned go = WS_KVP + (c * 32 + th) * 2176 + lane * 16;
      u32x4 pv[16][3];
#pragma unroll
      for (int t = 0; t < 16; ++t)
#pragma unroll
        for (int q = 0; q < 2; ++q) pv[t][q] = __builtin_amdgcn_raw_buffer_load_b128(rs, go + t * 4352 + q * 1024, 0, SC1);
      const unsigned gz = WS_KVP + (c * 32 + th) * 2176 + 2048 + (lane & 7) * 16;
#pragma unroll
      for (int t = 0; t < 16; ++t) pv[t][2] = __builtin_amdgcn_raw_buffer_load_b128(rs, gz + t * 4352, 0, SC1);
      SB();
      LAS char* kvf = lds + L_KVF + th * 2048;
#pragma unroll
      for (int q = 0; q < 3; ++q) {
        f32x4 a0 = __builtin_bit_cast(f32x4, pv[0][q]);
#pragma unroll
        for (int t = 1; t < 16; ++t) a0 += __builtin_bit_cast(f32x4, pv[t][q]);
        const int fl = q < 2 ? q * 32 + (lane >> 2) : ((lane >> 2) & 1) * 32 + 16;
        *(LAS u32x2*)(kvf + ((lane >> 1) & 1) * 1024 + fl * 16 + (lane & 1) * 8) = u32x2{pk2(a0[0], a0[1]), pk2(a0[2], a0[3])};
      }
      WAITL0();
      const bf16x8 k0 = lds_rd128(kvf + (h * 32 + min(r, 16)) * 16), k1 = lds_rd128(kvf + 1024 + (h * 32 + min(r, 16)) * 16);
      lds_wait_ge(lds, C_PQ, 4);
#pragma unroll
      for (int t = 0; t < 4; ++t) {
        LAS char* pq = lds + L_SCR + (t * 2 + th) * 2048 + lane * 16;
        f32x16 nm = MFMA(k0, lds_rd128(pq), zero);
        nm = MFMA(k1, lds_rd128(pq + 1024), nm);
        const float rd = __builtin_amdgcn_rcpf(nm[8] + 1e-6f);
        __builtin_amdgcn_raw_buffer_store_b128(__builtin_bit_cast(u32x4, pack_regs_scaled<0>(nm, rd)), rs,
                                               WS_O + ((4 * ti + t) * 32 + 2 * c + th) * 1024 + lane * 16, 0, SC1);
      }
      WAITV(0);
      publish(lds, C_O, 2, ws, WS_FO + (ti * 16 + c) * FS, tag64, lane);
      STAMP(8);
    }
  } else {
    auto w2c = [&](int k) {
      const int G = 26 + (w >> 1) + 2 * k;
      glds16(slot_src(G, wdown, wo) + th * 8192, wl, (unsigned)(size_t)lds + slot_lds(G) + th * 1024);
    };
    w2c(0); w2c(1); w2c(2); w2c(3);
    put_h(geglu8(st.acc[0], rstd[0]), 0, 2 * cg);
    SB();
    w2c(4); w2c(5); w2c(6); w2c(7);
    put_h(geglu8(st.acc[1], rstd[0]), 0, 2 * cg + 1);
    put_h(geglu8(st.acc[2], rstd[1]), 1, 2 * cg);
    put_h(geglu8(st.acc[3], rstd[1]), 1, 2 * cg + 1);
    SB();
    STAMP(5);
    f32x16 p0 = MFMA(wfrag(0), pack_regs_scaled<0>(st.acc[4], rstd[0]), zero);
    f32x16 p1 = MFMA(wfrag(1), pack_regs_scaled<8>(st.acc[4], rstd[0]), zero);
    dual_softmax(p0);
    dual_softmax(p1);
    SB();
    WAITV(0);
    publish(lds, C_H, 8, ws, WS_FH + (ti * 16 + c) * FS, tag64, lane);
    STAMP(6);
    lds_wait_ge(lds, C_KV, 4);
    LAS bf16x8* pq = (LAS bf16x8*)(lds + L_SCR + (2 * th + u0) * 4096 + lane * 16);
    pq[0] = pack_regs<0>(p0); pq[64] = pack_regs<8>(p0); pq[128] = pack_regs<0>(p1); pq[192] = pack_regs<8>(p1);
    WAITL0();
    lds_arrive(lds, C_PQ, lane);
    STAMP(8);
  }

  {
#pragma unroll
    for (int i = 0; i < 13; ++i) wr[i] = *(const GAS u32x4*)(wsrc + i * 16384 + wl);
    xr = *(const GAS u32x4*)(x + xoff);
    SB();
    if (isS) poll16(ws, WS_FO + (t2 >> 1) * 16 * FS, 0, 15, tag64, lane);
    else poll16(ws, WS_FH + (t2 >> 1) * 16 * FS, kq == 0 ? 0 : (kq == 1 ? 4 : 9), kq == 0 ? 4 : (kq == 1 ? 9 : 14), tag64, lane);
    STAMP(9);
    if (isS && blockIdx.x == 0 && threadIdx.x == 448) *(GAS unsigned*)ws = tag;
    const unsigned ao = (isS ? WS_O + (2 * t2 + tg) * 32 * 1024 : WS_H + ((2 * t2 + tg) * 86 + st0) * 1024) + lane * 16;
    u32x4 a[32];
    const unsigned ao2 = ao + (isS ? 3072 : 0);
#pragma unroll
    for (int i = 0; i < 16; ++i) a[i] = __builtin_amdgcn_raw_buffer_load_b128(rs, ao + i * 1024, 0, SC1);
    if (isS) {
#pragma unroll
      for (int i = 29; i < 32; ++i) a[i] = __builtin_amdgcn_raw_buffer_load_b128(rs, ao + (i - 13) * 1024, 0, SC1);
    }
#pragma unroll
    for (int i = 16; i < 29; ++i) a[i] = __builtin_amdgcn_raw_buffer_load_b128(rs, ao2 + i * 1024, 0, SC1);
    SB();
    const int q = (lane & 15) >> 2, p = lane & 3, g = (lane >> 4) & 1;
    LAS char* img = lds + slot_lds(16 * kq);
    LAS char* tr[2];
#pragma unroll
    for (int cc = 0; cc < 2; ++cc) tr[cc] = img + (4 * h + q) * 128 + (((4 * cc + 2 * g + (p >> 1)) ^ ((q >> 1) << 2)) << 4) + (p & 1) * 8;
    f32x16 acc[2] = {zero, zero};
    lds_wait_ge(lds, C_H, 8);
    if (kq == 2) a[0] = u32x4{0u, 0u, 0u, 0u};
    auto step = [&](int i, int slot) {
      const bf16x8 w0 = lds_tr2(tr[0] + slot * 2048, tr[0] + slot * 2048 + 1024);
      const bf16x8 w1 = lds_tr2(tr[1] + slot * 2048, tr[1] + slot * 2048 + 1024);
      acc[0] = MFMA(w0, __builtin_bit_cast(bf16x8, a[i]), acc[0]);
      acc[1] = MFMA(w1, __builtin_bit_cast(bf16x8, a[i]), acc[1]);
    };
#pragma unroll
    for (int i = 0; i < 13; ++i) step(i, i);
    WAITL0();
    lds_arrive(lds, C_R1 + w, lane);
    lds_wait_ge(lds, C_R1 + (w ^ 1), 1);
#pragma unroll
    for (int k = 0; k < 13; ++k) *(LAS u32x4*)(img + k * 2048 + tg * 1024 + lane * 16) = wr[k];
    WAITL0();
    lds_arrive(lds, C_R2 + w, lane);
#pragma unroll
    for (int i = 13; i < 16; ++i) step(i, i);
    if (isS) { step(29, 16); step(30, 17); step(31, 18); }
    lds_wait_ge(lds, C_R2 + (w ^ 1), 1);
    STAMP(10);
#pragma unroll
    for (int i = 16; i < 29; ++i) step(i, i - 16);
    STAMP(11);
    __syncthreads();
    {
      LAS char* row = lds + (kq * 64 + tg * 32 + r) * 272 + h * 16;
#pragma unroll
      for (int cc = 0; cc < 2; ++cc)
#pragma unroll
        for (int k = 0; k < 4; ++k)
          *(LAS f32x4*)(row + cc * 128 + k * 32) = f32x4{acc[cc][4 * k], acc[cc][4 * k + 1], acc[cc][4 * k + 2], acc[cc][4 * k + 3]};
    }
    __syncthreads();
    {
      LAS char* rp = lds + (threadIdx.x >> 3) * 272 + (threadIdx.x & 7) * 32;
      float res[8];
#pragma unroll
      for (int hf = 0; hf < 2; ++hf) {
        const f32x4 gg = *(LAS f32x4*)(rp + hf * 16) + *(LAS f32x4*)(rp + 64 * 272 + hf * 16) + *(LAS f32x4*)(rp + 128 * 272 + hf * 16);
        const f32x4 ss = *(LAS f32x4*)(rp + 192 * 272 + hf * 16);
#pragma unroll
        for (int e = 0; e < 4; ++e) {
          const unsigned xw = xr[hf * 2 + (e >> 1)];
          const float xv = asf((e & 1) ? (xw & 0xffff0000u) : (xw << 16));
          const float sg = gg[e] * __builtin_amdgcn_rcpf(1.f + __builtin_amdgcn_exp2f(-gg[e] * LOG2E));
          res[hf * 4 + e] = __builtin_fmaf(ss[e], sg, xv);
        }
      }
      *(GAS bf16x8*)(out + xoff) = pack8(res[0], res[1], res[2], res[3], res[4], res[5], res[6], res[7]);
    }
    STAMP(12);
  }
  DIAG_END();
}

extern "C" void kernel_launch(void* const* d_in, const int* in_sizes, int n_in, void* d_out, int out_size, void* d_ws, size_t ws_size,
                              hipStream_t stream) {
  (void)hipFuncSetAttribute((const void*)fused_block, hipFuncAttributeMaxDynamicSharedMemorySize, LDS_BYTES);
  fused_block<<<256, 512, LDS_BYTES, stream>>>((const char*)d_in[0], (const char*)d_in[1], (const char*)d_in[2], (const char*)d_in[3],
                                               (const char*)d_in[5], (const char*)d_in[6], (const char*)d_in[7], (char*)d_out, (char*)d_ws);
}
```

```cpp
#include <hip/hip_runtime.h>
#include <hip/hip_bf16.h>

#define LAS __attribute__((address_space(3)))
#define GAS __attribute__((address_space(1)))
#define DI __device__ __forceinline__
typedef short bf16x8 __attribute__((ext_vector_type(8)));
typedef short s16x4 __attribute__((ext_vector_type(4)));
typedef float f32x16 __attribute__((ext_vector_type(16)));
typedef float f32x4 __attribute__((ext_vector_type(4)));
typedef float f32x2 __attribute__((ext_vector_type(2)));
typedef unsigned u32x4 __attribute__((ext_vector_type(4)));
typedef unsigned u32x2 __attribute__((ext_vector_type(2)));
typedef __bf16 bf2 __attribute__((ext_vector_type(2)));
typedef unsigned long long u64;

constexpr int DM = 512, FF = 1376;
constexpr int XB = 16384, RG = 12288, GOFF = XB, UOFF = XB + RG + 32, QOFF = XB + 2 * RG + 32, STG = XB + 3 * RG + 32;
constexpr int L_WPH = 3 * STG, L_CNT = L_WPH + 1024, L_COL = L_CNT + 256;
constexpr int L_SCR = 61440, SCR_BYTES = 20992, L_KVF = L_SCR + 16384;
constexpr int LDS_BYTES = 163840;
constexpr int L_DUMMY = L_COL + 2560;
static_assert(L_DUMMY + 64 <= LDS_BYTES, "lds");
enum { C_KV = 0, C_H = 1, C_O = 2, C_PQ = 3, C_EX = 4  , C_R1 = 8  , C_R2 = 16  , C_R3 = 24  , C_G = 30, C_GD = 31, C_S = 32, C_XD = 33, C_W = 34 };
constexpr unsigned FS = 256;
constexpr unsigned WS_ST = 4096, WS_FKV = 65536, WS_FH = 2 * 65536, WS_FO = 3 * 65536;
constexpr unsigned WS_H = 4 * 65536;
constexpr unsigned WS_O = WS_H + 64u * 86 * 1024;
constexpr unsigned WS_KVP = WS_O + 64u * 32 * 1024;
constexpr unsigned WS_END = WS_KVP + 512u * 2176;
#define PSLEEP 2
constexpr int SC1 = 16;
constexpr unsigned SPIN_G = 1u << 15, SPIN_L = 1u << 19;

#define STAMP(i) do {} while (0)
#define DIAG_BEGIN() do {} while (0)
#define DIAG_END() do {} while (0)
#define DGARG
#define DGPASS

#define MFMA(a, b, c) __builtin_amdgcn_mfma_f32_32x32x16_bf16((a), (b), (c), 0, 0, 0)
#define WAITV(n) asm volatile("s_waitcnt vmcnt(" #n ")" ::: "memory")
#define WAITL0() asm volatile("s_waitcnt lgkmcnt(0)" ::: "memory")
#define BAR() __builtin_amdgcn_s_barrier()
#define SB() __builtin_amdgcn_sched_barrier(0)
#define RLX_AGENT __ATOMIC_RELAXED, __HIP_MEMORY_SCOPE_AGENT
#define RLX_WG __ATOMIC_RELAXED, __HIP_MEMORY_SCOPE_WORKGROUP

DI float asf(unsigned u) { return __builtin_bit_cast(float, u); }
DI unsigned asu(float f) { return __builtin_bit_cast(unsigned, f); }
DI unsigned pk2(float a, float b) {
  f32x2 f = {a, b};
  return __builtin_bit_cast(unsigned, __builtin_convertvector(f, bf2));
}
DI bf16x8 pack8(float a0, float a1, float a2, float a3, float a4, float a5, float a6, float a7) {
  u32x4 p = {pk2(a0, a1), pk2(a2, a3), pk2(a4, a5), pk2(a6, a7)};
  return __builtin_bit_cast(bf16x8, p);
}
template <int S0> DI bf16x8 pack_regs(const f32x16& x) {
  return pack8(x[S0], x[S0 + 1], x[S0 + 2], x[S0 + 3], x[S0 + 4], x[S0 + 5], x[S0 + 6], x[S0 + 7]);
}
template <int S0> DI bf16x8 pack_regs_scaled(const f32x16& x, float s) {
  return pack8(x[S0] * s, x[S0 + 1] * s, x[S0 + 2] * s, x[S0 + 3] * s, x[S0 + 4] * s, x[S0 + 5] * s, x[S0 + 6] * s, x[S0 + 7] * s);
}
DI float bf_hi(float v) { return asf(pk2(0.f, v) & 0xffff0000u); }
DI void glds16(const char* sbase, unsigned voff, unsigned ldst) {
  asm volatile("s_mov_b32 m0, %2\n\ts_nop 0\n\tglobal_load_lds_dwordx4 %0, %1" ::"v"(voff), "s"(sbase), "s"(ldst) : "memory");
}
DI void warm10(const char* lane_ptr, unsigned ldst) {
  asm volatile("s_mov_b32 m0, %1\n\ts_mov_b64 exec, 0x3ff\n\tglobal_load_lds_dword %0, off\n\ts_mov_b64 exec, -1" ::"v"(lane_ptr), "s"(ldst) : "memory");
}
DI bf16x8 lds_rd128(LAS char* p) { return *(LAS bf16x8*)p; }
DI bf16x8 lds_tr2(LAS char* p0, LAS char* p1) {
  s16x4 lo = __builtin_amdgcn_ds_read_tr16_b64_v4i16((LAS s16x4*)p0);
  s16x4 hi = __builtin_amdgcn_ds_read_tr16_b64_v4i16((LAS s16x4*)p1);
  return __builtin_shufflevector(lo, hi, 0, 1, 2, 3, 4, 5, 6, 7);
}
DI void acc_pair(unsigned u, float& a0, float& a1) {
  unsigned t0, t1;
  asm volatile("v_lshlrev_b32 %2, 16, %4\n\tv_and_b32 %3, %5, %4\n\tv_add_f32 %0, %0, %2\n\tv_add_f32 %1, %1, %3"
               : "+v"(a0), "+v"(a1), "=&v"(t0), "=&v"(t1) : "v"(u), "s"(0xffff0000u));
}
#define MX(a, b) __builtin_elementwise_maximum((a), (b))
DI float max_x32(float v) {
  auto r = __builtin_amdgcn_permlane32_swap(asu(v), asu(v), false, false);
  const unsigned r0 = r[0], r1 = r[1];
  return MX(asf(r0), asf(r1));
}
DI float add_x32(float v) {
  auto r = __builtin_amdgcn_permlane32_swap(asu(v), asu(v), false, false);
  const unsigned r0 = r[0], r1 = r[1];
  return asf(r0) + asf(r1);
}
template <int CTRL> DI float dpp_add(float v) { return v + asf(__builtin_amdgcn_update_dpp(0u, asu(v), CTRL, 0xf, 0xf, true)); }
DI float wave_sum(float v) {
  v = dpp_add<0xB1>(v); v = dpp_add<0x4E>(v); v = dpp_add<0x141>(v); v = dpp_add<0x140>(v);
  auto r = __builtin_amdgcn_permlane16_swap(asu(v), asu(v), false, false);
  const unsigned r0 = r[0], r1 = r[1];
  return add_x32(asf(r0) + asf(r1));
}
constexpr float LOG2E = 1.4426950408889634f;

DI void dual_softmax(const f32x16& p, bf16x8 (&o)[2]) {
  float m[2];
#pragma unroll
  for (int g = 0; g < 2; ++g)
    m[g] = MX(MX(MX(MX(p[8 * g], p[8 * g + 1]), p[8 * g + 2]), MX(MX(p[8 * g + 3], p[8 * g + 4]), p[8 * g + 5])), MX(p[8 * g + 6], p[8 * g + 7]));
  f32x2 e[2][4];
#pragma unroll
  for (int g = 0; g < 2; ++g) {
    const float mm = max_x32(m[g]) * -LOG2E;
#pragma unroll
    for (int i = 0; i < 4; ++i) {
      const f32x2 t = __builtin_elementwise_fma(f32x2{p[8 * g + 2 * i], p[8 * g + 2 * i + 1]}, f32x2{LOG2E, LOG2E}, f32x2{mm, mm});
      e[g][i] = f32x2{__builtin_amdgcn_exp2f(t[0]), __builtin_amdgcn_exp2f(t[1])};
    }
  }
#pragma unroll
  for (int g = 0; g < 2; ++g) {
    const f32x2 s2 = (e[g][0] + e[g][1]) + (e[g][2] + e[g][3]);
    const float rc = __builtin_amdgcn_rcpf(add_x32(s2[0] + s2[1]));
    u32x4 q;
#pragma unroll
    for (int i = 0; i < 4; ++i) q[i] = __builtin_bit_cast(unsigned, __builtin_convertvector(e[g][i] * rc, bf2));
    o[g] = __builtin_bit_cast(bf16x8, q);
  }
}
DI u32x4 geglu8(const f32x16& acc, float rs) {
  const float c1 = -2.f * 0.7978845608028654f * LOG2E, c2 = c1 * 0.044715f;
  const float k1 = c1 * rs, rs2 = rs * rs, k2 = c2 * rs2 * rs, ir2 = __builtin_amdgcn_rcpf(rs2);
  u32x4 o;
#pragma unroll
  for (int i = 0; i < 4; ++i) {
    const f32x2 a = {acc[2 * i], acc[2 * i + 1]}, b = {acc[8 + 2 * i], acc[9 + 2 * i]};
    const f32x2 t = a * __builtin_elementwise_fma(a * a, f32x2{k2, k2}, f32x2{k1, k1});
    const f32x2 ex = {__builtin_amdgcn_exp2f(t[0]), __builtin_amdgcn_exp2f(t[1])};
    const f32x2 d = __builtin_elementwise_fma(ex, f32x2{ir2, ir2}, f32x2{ir2, ir2});
    const f32x2 rc = {__builtin_amdgcn_rcpf(d[0]), __builtin_amdgcn_rcpf(d[1])};
    o[i] = __builtin_bit_cast(unsigned, __builtin_convertvector((a * b) * rc, bf2));
  }
  return o;
}
DI void lds_wait_ge(LAS char* lds, int idx, unsigned want) {
  volatile LAS unsigned* p = (volatile LAS unsigned*)(lds + L_CNT) + idx;
#pragma unroll 1
  for (unsigned s = 0; s < SPIN_L; ++s) {
    if (__builtin_amdgcn_readfirstlane(*p) >= want) break;
    __builtin_amdgcn_s_sleep(1);
  }
  asm volatile("" ::: "memory");
}
DI unsigned lds_arrive(LAS char* lds, int idx, int lane) {
  unsigned old = 0;
  if (lane == 0) old = __hip_atomic_fetch_add((LAS unsigned*)(lds + L_CNT) + idx, 1u, RLX_WG);
  return __builtin_amdgcn_readfirstlane(old);
}
DI void publish(LAS char* lds, int idx, unsigned n, char* ws, unsigned flag_off, u64 tag64, int lane) {
  if (lds_arrive(lds, idx, lane) == n - 1 && lane == 0) __hip_atomic_store((GAS u64*)(ws + flag_off), tag64, RLX_AGENT);
}
DI void poll16(const char* ws, unsigned off, int lo, int hi, u64 tag64, int lane) {
  const int l = min(max(lane & 15, lo), hi);
  GAS u64* p = (GAS u64*)(ws + off + l * FS);
#pragma unroll 1
  for (unsigned s = 0; s < SPIN_G; ++s) {
    const u64 v = __hip_atomic_load(p, RLX_AGENT);
    if (__all(v == tag64)) break;
    __builtin_amdgcn_s_sleep(PSLEEP);
  }
  __builtin_amdgcn_fence(__ATOMIC_ACQUIRE, "wavefront");
}

template <bool V> struct BoolC { static constexpr bool value = V; };
template <int V> struct IntC { static constexpr int value = V; };
struct Frags { bf16x8 x[2]; bf16x8 w[3]; };
struct P1State { f32x16 acc[5]; float cs[3][2]; float rstd[2]; bf16x8 mf[2], wf[2], idf[2]; unsigned tag; };

DI unsigned slot_lds(int G) { return 2048u * G + (G >= 30 ? (unsigned)SCR_BYTES : 0u); }
DI int rot29(int j, int cj) {
  const int v = j + ((cj * 29) >> 3); return v >= 29 ? v - 29 : v; }
DI int rot32(int j, int cj) {
  return (j + 4 * cj) & 31; }
DI const char* slot_src(int G, const char* wdown, const char* wo, int cj) {
  const int p = (G >= 15) + (G >= 30);
  return G < 45 ? wdown + (size_t)(29 * p - (p >> 1) + rot29(G - 15 * p, cj)) * 16384 : wo + (size_t)rot32(G - 45, cj) * 16384;
}
DI void gload16(u32x4& dst, const char* sbase, unsigned voff) {
  asm volatile("global_load_dwordx4 %0, %1, %2" : "=v"(dst) : "v"(voff), "s"(sbase) : "memory");
}

template <bool LO>
DI void p1_mainloop(P1State& st, LAS char* lds, const char* xg, const char* wG, const char* wU, const char* wQ, const char* wdown, const char* wo,
                    char* ws, int ti, int c, unsigned wl, int w, int lane, int e, int cj DGARG) {
  const int th = w & 1, cg = w >> 1, r = lane & 31, h = lane >> 5;
  const unsigned lb = (unsigned)(size_t)lds;
  u32x4 xrow;
  const int myrow = 8 * c + w;
  asm volatile("global_load_dwordx4 %0, %1, %2" : "=v"(xrow) : "v"(myrow * 1024 + lane * 16), "s"(xg) : "memory");
  unsigned xo[2], go[2], qo[2];
#pragma unroll
  for (int a = 0; a < 2; ++a) {
    const int row = 8 * (2 * w + a) + (lane >> 3);
    xo[a] = row * 1024 + (((lane & 7) ^ ((row >> 1) & 7)) << 4);
    const int p = a == 0 ? w : 8 + (w & 3);
    const int q = 64 * p + lane, rr = q / 12, cc = q - rr * 12;
    go[a] = rr * (2 * FF) + cc * 16;
    const int mat = cc < 8 ? 1 + ((cc >> 1) & 1) : 0, hh = cc < 8 ? cc >> 2 : (cc - 8) >> 1;
    qo[a] = rr * 3072 + mat * 1024 + hh * 32 + (cc & 1) * 16;
  }
  const int p2 = 8 + (w & 3);
  auto piece = [&](int k, int s, unsigned bo) {
    const unsigned b = lb + bo;
    if (k == 0) glds16(xg + s * 128, xo[0], b + 2 * w * 1024);
    if (k == 1) glds16(wG + s * (128 * FF), go[0], b + GOFF + w * 1024);
    if (k == 2) glds16(wU + s * (128 * FF), go[0], b + UOFF + w * 1024);
    if (k == 3) glds16(wQ + s * (64 * 3072), qo[0], b + QOFF + w * 1024);
    if (k == 4) glds16(xg + s * 128, xo[1], b + (2 * w + 1) * 1024);
    if (k == 5) glds16((LO ? wG : wU) + s * (128 * FF), go[1], b + (LO ? GOFF : UOFF) + p2 * 1024);
    if (k == 6 && !LO) glds16(wQ + s * (64 * 3072), qo[1], b + QOFF + p2 * 1024);
  };
  auto w2piece = [&](int k, int G0, int nslot) {
    const int L = (w >> 1) + 4 * k;
    if (4 * k + 3 < nslot || L < nslot) glds16(slot_src(G0 + L, wdown, wo, cj) + th * 8192, wl, lb + slot_lds(G0 + L) + th * 1024);
  };
#pragma unroll
  for (int k = 0; k < 7; ++k) piece(k, 0, 0);
  BAR();
#pragma unroll
  for (int k = 0; k < 7; ++k) piece(k, 1, STG);
  unsigned tag;
  asm volatile("s_load_dword %0, %1, 0x0" : "=s"(tag) : "s"(ws) : "memory");
  const int rot = LO ? 2 * th + cg : 2 * th;
  const int g = (lane >> 4) & 1, u0 = LO ? cg : 0;
  const int wrow = (8 * h + ((lane & 15) >> 2)) * 192 + (lane & 3) * 8;
  const int wFF = (g ? UOFF : GOFF) + wrow + (LO ? 64 * cg : 32 * (cg + 2));
  const int wQK = QOFF + wrow + g * 32 + (LO ? 128 : 64 * (cg - 2));
  int xad[4], kof[4];
#pragma unroll
  for (int kk = 0; kk < 4; ++kk) {
    const int ks = (kk + rot) & 3;
    xad[kk] = (64 * th + r) * 128 + (((2 * ks + h) ^ ((r >> 1) & 7)) << 4);
    kof[kk] = ks * 3072;
  }
  const int xa0 = u0 * 4096, xa1 = 4096 - xa0;
  auto load_part = [&](Frags& f, int bo, int kk, int part) {
    LAS char* xa = lds + bo + xad[kk];
    LAS char* a = lds + (bo + kof[kk]) + wFF;
    LAS char* q = lds + (bo + kof[kk]) + wQK;
    if (part == 0) { f.x[0] = lds_rd128(xa + xa0); f.w[0] = LO ? lds_tr2(a, a + 768) : lds_tr2(q, q + 768); }
    if (part == 1) f.w[1] = LO ? lds_tr2(a + 32, a + 32 + 768) : lds_tr2(a, a + 768);
    if (part == 2) f.x[1] = lds_rd128(xa + xa1);
    if (part == 3 && LO) f.w[2] = lds_tr2(q, q + 768);
  };
#pragma unroll
  for (int i = 0; i < 5; ++i)
#pragma unroll
    for (int j = 0; j < 16; ++j) st.acc[i][j] = (!LO && i == 4 && r >= 16) ? 1.f : 0.f;
  if (!LO) {
#pragma unroll
    for (int s = 0; s < 2; ++s)
#pragma unroll
      for (int j = 0; j < 8; ++j) st.idf[s][j] = (16 * s + 8 * (j >> 2) + 4 * h + (j & 3) == r) ? (short)0x3f80 : (short)0;
    asm volatile("" : "+v"(st.idf[0]), "+v"(st.idf[1]), "+v"(st.acc[4]));
  }
#pragma unroll
  for (int i = 0; i < 3; ++i) st.cs[i][0] = st.cs[i][1] = 0.f;
  if (LO) asm volatile("s_waitcnt vmcnt(12) lgkmcnt(0)" : "+v"(xrow), "+s"(tag)::"memory");
  else asm volatile("s_waitcnt vmcnt(14) lgkmcnt(0)" : "+v"(xrow), "+s"(tag)::"memory");
  tag += 1u;
  st.tag = tag;
  {
    float v[8];
#pragma unroll
    for (int i = 0; i < 4; ++i) { const unsigned t = xrow[i]; v[2 * i] = asf(t << 16); v[2 * i + 1] = asf(t & 0xffff0000u); }
    const float mean = wave_sum(((v[0] + v[1]) + (v[2] + v[3])) + ((v[4] + v[5]) + (v[6] + v[7]))) * (1.f / DM);
    float ss = 0.f;
#pragma unroll
    for (int i = 0; i < 8; ++i) { const float d = v[i] - mean; ss = __builtin_fmaf(d, d, ss); }
    const float rstd = __builtin_amdgcn_rsqf(wave_sum(ss) * (1.f / DM) + 1e-5f);
    const u32x4 gr = {asu(mean), tag, asu(rstd), tag};
    if (lane == 0)
      asm volatile("global_store_dwordx4 %0, %1, %2 sc1\n\ts_nop 1" ::"v"(WS_ST + (ti * 128 + myrow) * 16), "v"(gr), "s"(ws) : "memory");
  }
  STAMP(1);
  Frags f[2];
  u32x4 stt[2];
  auto mma = [&](const Frags& cf, auto kk_tag, auto&& rd, auto&& dma0, auto&& dma1) {
    constexpr int kk = decltype(kk_tag)::value;
    const u32x4 cw = __builtin_bit_cast(u32x4, cf.w[kk < 2 ? 0 : 1]);
    float(&cs)[2] = st.cs[kk < 2 ? 0 : 1];
    SB();
    st.acc[0] = MFMA(cf.w[0], cf.x[0], st.acc[0]); SB(); rd(0); acc_pair(cw[0], cs[0], cs[1]); SB();
    st.acc[1] = MFMA(cf.w[1], cf.x[0], st.acc[1]); SB(); rd(1); acc_pair(cw[1], cs[0], cs[1]); dma0(); SB();
    st.acc[2] = MFMA(cf.w[0], cf.x[1], st.acc[2]); SB(); rd(2); acc_pair(cw[2], cs[0], cs[1]); SB();
    st.acc[3] = MFMA(cf.w[1], cf.x[1], st.acc[3]); SB(); rd(3); acc_pair(cw[3], cs[0], cs[1]); dma1(); SB();
    if (LO) {
      st.acc[4] = MFMA(cf.w[2], cf.x[0], st.acc[4]);
      if (kk == 0) {
        const u32x4 cq = __builtin_bit_cast(u32x4, cf.w[2]);
#pragma unroll
        for (int i = 0; i < 4; ++i) acc_pair(cq[i], st.cs[2][0], st.cs[2][1]);
      }
      SB();
    }
  };
  auto wfrag = [&](int hh) {
    LAS char* p = lds + L_WPH + hh * 512 + (4 * h + ((lane & 15) >> 2)) * 32 + (lane & 3) * 8;
    s16x4 lo = __builtin_amdgcn_ds_read_tr16_b64_v4i16((LAS s16x4*)p);
    s16x4 hi = __builtin_amdgcn_ds_read_tr16_b64_v4i16((LAS s16x4*)(p + 256));
    const unsigned neg = (lane & 16) ? 0x80008000u : 0u;
    u32x2 a = __builtin_bit_cast(u32x2, lo), b = __builtin_bit_cast(u32x2, hi);
    return __builtin_bit_cast(bf16x8, u32x4{a[0] ^ neg, a[1] ^ neg, b[0] ^ neg, b[1] ^ neg});
  };
  int cur = 0;
  auto iter = [&](int s, auto mode_tag) {
    constexpr int MODE = decltype(mode_tag)::value;
    const int fre = (cur == 0) ? 2 * STG : cur - STG;
    auto dma = [&](int k) {
      if (MODE <= 1) piece(k, s + 2, fre);
      if (MODE == 2) w2piece(k, 42, 25);
      if (MODE == 3) w2piece(k, 0, 26);
    };
    WAITL0();
    if (MODE == 3) asm volatile("s_waitcnt vmcnt(6)" : "+v"(stt[0]), "+v"(stt[1])::"memory");
    else WAITV(6);
    BAR();
    if (MODE == 0) STAMP(2);
    if (MODE == 2) {
#pragma unroll
      for (int u = 0; u < 2; ++u)
        asm volatile("global_load_dwordx4 %0, %1, %2 sc1" : "=v"(stt[u]) : "v"(WS_ST + (ti * 128 + 64 * th + 32 * (u ^ u0) + r) * 16), "s"(ws) : "memory");
    }
    if (MODE != 0) mma(f[1], IntC<3>{}, [&](int p) { load_part(f[0], cur, 0, p); }, [&] { dma(0); }, [&] { dma(1); });
    else { load_part(f[0], cur, 0, 0); load_part(f[0], cur, 0, 1); load_part(f[0], cur, 0, 2); load_part(f[0], cur, 0, 3); dma(0); dma(1); }
    mma(f[0], IntC<0>{}, [&](int p) { load_part(f[1], cur, 1, p); }, [&] { dma(2); }, [&] { dma(3); });
    if (MODE == 3) {
#pragma unroll 1
      for (unsigned sp = 0; sp < SPIN_G; ++sp) {
        if (__all((((stt[0][1] ^ tag) | (stt[0][3] ^ tag)) | ((stt[1][1] ^ tag) | (stt[1][3] ^ tag))) == 0u)) break;
        asm volatile("global_load_dwordx4 %0, %2, %4 sc1\n\tglobal_load_dwordx4 %1, %3, %4 sc1\n\ts_waitcnt vmcnt(0)"
                     : "=&v"(stt[0]), "=&v"(stt[1])
                     : "v"(WS_ST + (ti * 128 + 64 * th + 32 * u0 + r) * 16), "v"(WS_ST + (ti * 128 + 64 * th + 32 * (1 ^ u0) + r) * 16), "s"(ws)
                     : "memory");
      }
#pragma unroll
      for (int u = 0; u < 2; ++u) {
        const unsigned mu = stt[u][0], ru = stt[u][2];
        const float mean = h ? 0.f : -asf(mu);
        st.rstd[u] = asf(ru);
        const float mh = bf_hi(mean);
        st.mf[u] = __builtin_bit_cast(bf16x8, u32x4{pk2(mh, mean - mh), pk2(mh, 0.f), 0u, 0u});
      }
      st.wf[0] = wfrag(LO ? 0 : cg - 2);
      if (LO) st.wf[1] = wfrag(1);
      asm volatile("" : "+v"(st.mf[0]), "+v"(st.mf[1]), "+v"(st.wf[0]));
      if (LO) asm volatile("" : "+v"(st.wf[1]));
    }
    mma(f[1], IntC<1>{}, [&](int p) { load_part(f[0], cur, 2, p); }, [&] { dma(4); }, [&] { dma(5); });
    mma(f[0], IntC<2>{}, [&](int p) { load_part(f[1], cur, 3, p); }, [&] { dma(6); }, [] {});
    cur = (cur == 2 * STG) ? 0 : cur + STG;
  };
  iter(0, IntC<0>{});
#pragma unroll 1
  for (int s = 1; s < 6; ++s) iter(s, IntC<1>{});
  iter(6, IntC<2>{});
  iter(7, IntC<3>{});
  mma(f[1], IntC<3>{}, [](int) {}, [] {}, [] {});
  STAMP(3);
  LAS float* col = (LAS float*)(lds + L_COL);
#pragma unroll
  for (int i = 0; i < (LO ? 3 : 2); ++i) {
    const float v = add_x32(st.cs[i][0] + st.cs[i][1]);
    col[(LO ? w * 3 + i : 12 + (w - 4) * 2 + i) * 32 + r] = v;
  }
  WAITL0();
  BAR();
}

__global__ __launch_bounds__(512, 2) void fused_block(const char* x, const char* wqkv, const char* wphi,
                                                      const char* wo, const char* wgate, const char* wup,
                                                      const char* wdown, char* out, char* ws) {
  extern __shared__ __attribute__((aligned(16))) char smem[];
  LAS char* lds = (LAS char*)smem;
  DIAG_BEGIN();
  asm volatile("" : "+s"(x), "+s"(wqkv), "+s"(wphi), "+s"(wo), "+s"(wgate), "+s"(wup), "+s"(wdown), "+s"(out), "+s"(ws));
  const int lane = threadIdx.x & 63, w = __builtin_amdgcn_readfirstlane(threadIdx.x >> 6);
  const int xcd = blockIdx.x & 7, idx = blockIdx.x >> 3;
  const int ti = 8 * (xcd & 1) + (idx & 7), c = 4 * (xcd >> 1) + (idx >> 3);
  const int t2 = 4 * xcd + (idx & 3), cj = idx >> 2;
  const int th = w & 1, cg = w >> 1, r = lane & 31, h = lane >> 5;
  if (w == 0) glds16(wphi + c * 1024, lane * 16, (unsigned)(size_t)lds + L_WPH);
  if (w == 1) ((LAS unsigned*)(lds + L_CNT))[lane] = 0u;
  const int f0 = min(96 * c, FF - 96);
  const char* xg = x + (size_t)ti * 128 * 1024;
  const unsigned wl = (lane >> 3) * 1024 + cj * 128 + (((lane & 7) ^ (((lane >> 4) & 1) << 2)) << 4);
  const __amdgpu_buffer_rsrc_t rs = __builtin_amdgcn_make_buffer_rsrc((void*)ws, 0, (int)WS_END, 0x00020000);
  P1State st;
  if (w < 4) p1_mainloop<true>(st, lds, xg, wgate + 2 * f0, wup + 2 * f0, wqkv + c * 64, wdown, wo, ws, ti, c, wl, w, lane, idx * 8 + w, cj DGPASS);
  else p1_mainloop<false>(st, lds, xg, wgate + 2 * f0, wup + 2 * f0, wqkv + c * 64, wdown, wo, ws, ti, c, wl, w, lane, idx * 8 + w, cj DGPASS);

  const unsigned tag = st.tag;
  const u64 tag64 = ((u64)tag << 32) | (u64)(~tag);
  const int u0 = w < 4 ? cg : 0;
  const float(&rstd)[2] = st.rstd;
  const bf16x8(&mf)[2] = st.mf;
  auto csfrag = [&](float cs) {
    const float ch = bf_hi(cs), cl = cs - ch;
    return __builtin_bit_cast(bf16x8, u32x4{pk2(ch, ch), pk2(cl, 0.f), 0u, 0u});
  };
  {
    LAS float* col = (LAS float*)(lds + L_COL);
    const int b0 = w < 4 ? (w & 2) * 3 : 12 + (w & 2) * 2, bs = w < 4 ? 3 : 2;
    const bf16x8 c0 = csfrag(col[b0 * 32 + r] + col[(b0 + bs) * 32 + r]);
    const bf16x8 c1 = csfrag(col[(b0 + 1) * 32 + r] + col[(b0 + bs + 1) * 32 + r]);
    st.acc[0] = MFMA(c0, mf[0], st.acc[0]);
    st.acc[2] = MFMA(c0, mf[1], st.acc[2]);
    st.acc[1] = MFMA(c1, mf[0], st.acc[1]);
    st.acc[3] = MFMA(c1, mf[1], st.acc[3]);
    if (w < 4) st.acc[4] = MFMA(csfrag((col[2 * 32 + r] + col[5 * 32 + r]) + (col[8 * 32 + r] + col[11 * 32 + r])), mf[0], st.acc[4]);
  }
  STAMP(4);
  f32x16 zero;
#pragma unroll
  for (int j = 0; j < 16; ++j) zero[j] = 0.f;
  const int jlo = c < 14 ? 0 : (c == 14 ? 4 : 6);
  auto put_h = [&](const u32x4& o, int u, int j) {
    if (j >= jlo) __builtin_amdgcn_raw_buffer_store_b128(o, rs, WS_H + ((4 * ti + 2 * th + (u ^ u0)) * 86 + (f0 >> 4) + j) * 1024 + lane * 16, 0, SC1);
  };
  const int tg = th, kq = cg;
  const bool isS = kq == 3;
  const int st0 = kq == 0 ? 0 : (kq == 1 ? 29 : 57);
  const char* wsrc = wdown + (size_t)st0 * 16384 + tg * 8192;
  u32x4 wr[14];
  auto ldw = [&](int i0, int i1) {
    SB();
#pragma unroll
    for (int i = i0; i < i1; ++i) gload16(wr[i], wsrc + rot29(15 + i, cj) * 16384, wl);
    SB();
  };
  const int q4 = (lane & 15) >> 2, p4 = lane & 3, g4 = (lane >> 4) & 1;
  int tro[2];
#pragma unroll
  for (int cc = 0; cc < 2; ++cc) tro[cc] = (4 * h + q4) * 128 + (((4 * cc + 2 * g4 + (p4 >> 1)) ^ ((q4 >> 1) << 2)) << 4) + (p4 & 1) * 8;
  f32x16 acc[2] = {zero, zero};
  auto steps = [&](auto i0_tag, auto i1_tag, const u32x4* a, auto&& slot) {
    constexpr int I0 = decltype(i0_tag)::value, I1 = decltype(i1_tag)::value, PD = 3;
    bf16x8 wf[PD + 1][2];
    auto rd = [&](int i) {
      LAS char* s = slot(i);
      wf[i % (PD + 1)][0] = lds_tr2(s + tro[0], s + tro[0] + 1024);
      wf[i % (PD + 1)][1] = lds_tr2(s + tro[1], s + tro[1] + 1024);
    };
#pragma unroll
    for (int i = I0; i < I0 + PD; ++i) rd(i);
    SB();
#pragma unroll
    for (int i = I0; i < I1; ++i) {
      acc[0] = MFMA(wf[i % (PD + 1)][0], __builtin_bit_cast(bf16x8, a[i]), acc[0]);
      SB();
      if (i + PD < I1) rd(i + PD);
      SB();
      acc[1] = MFMA(wf[i % (PD + 1)][1], __builtin_bit_cast(bf16x8, a[i]), acc[1]);
      SB();
    }
  };
  auto put_part = [&](LAS char* tile) {
    LAS char* row = tile + r * 272 + h * 16;
#pragma unroll
    for (int cc = 0; cc < 2; ++cc)
#pragma unroll
      for (int k = 0; k < 4; ++k)
        *(LAS f32x4*)(row + cc * 128 + k * 32) = f32x4{acc[cc][4 * k], acc[cc][4 * k + 1], acc[cc][4 * k + 2], acc[cc][4 * k + 3]};
    WAITL0();
  };
  if (w >= 4) {
    __builtin_amdgcn_s_setprio(2);
    const int hh = cg - 2;
    const bf16x8(&idf)[2] = st.idf;
    f32x16 p0 = MFMA(st.wf[0], pack_regs_scaled<0>(st.acc[0], rstd[0]), zero);
    f32x16 p1 = MFMA(st.wf[0], pack_regs_scaled<0>(st.acc[2], rstd[1]), zero);
    const f32x16 vt0 = MFMA(pack_regs_scaled<8>(st.acc[0], rstd[0]), idf[0], st.acc[4]);
    const f32x16 vt1 = MFMA(pack_regs_scaled<8>(st.acc[2], rstd[1]), idf[0], st.acc[4]);
    bf16x8 ph0[2], ph1[2];
    dual_softmax(p0, ph0);
    f32x16 pt0 = MFMA(ph0[0], idf[0], zero);
    pt0 = MFMA(ph0[1], idf[1], pt0);
    dual_softmax(p1, ph1);
    f32x16 pt1 = MFMA(ph1[0], idf[0], zero);
    pt1 = MFMA(ph1[1], idf[1], pt1);
    f32x16 kv = MFMA(pack_regs<0>(pt0), pack_regs<0>(vt0), zero);
    kv = MFMA(pack_regs<8>(pt0), pack_regs<8>(vt0), kv);
    kv = MFMA(pack_regs<0>(pt1), pack_regs<0>(vt1), kv);
    kv = MFMA(pack_regs<8>(pt1), pack_regs<8>(vt1), kv);
    LAS f32x4* ex = (LAS f32x4*)(lds + L_SCR);
    f32x4 give[2], keep[2];
#pragma unroll
    for (int q = 0; q < 2; ++q)
#pragma unroll
      for (int e = 0; e < 4; ++e) { give[q][e] = th ? kv[4 * q + e] : kv[8 + 4 * q + e]; keep[q][e] = th ? kv[8 + 4 * q + e] : kv[4 * q + e]; }
    ex[((w - 4) * 2) * 64 + lane] = give[0];
    ex[((w - 4) * 2 + 1) * 64 + lane] = give[1];
    WAITL0();
    lds_arrive(lds, C_EX + (w - 4), lane);
    lds_wait_ge(lds, C_EX + ((w - 4) ^ 1), 1);
    const unsigned po = WS_KVP + ((c * 16 + ti) * 2 + hh) * 2176 + (r < 16 ? h * 16 + r : 32 + h) * 64 + th * 32;
    if (r <= 16) {
#pragma unroll
      for (int q = 0; q < 2; ++q)
        __builtin_amdgcn_raw_buffer_store_b128(__builtin_bit_cast(u32x4, keep[q] + ex[(((w - 4) ^ 1) * 2 + q) * 64 + lane]), rs, po + q * 16, 0, SC1);
    }
    SB();
    lds_arrive(lds, C_XD, lane);
    STAMP(5);
    u32x4 h0 = geglu8(st.acc[1], rstd[0]), h1 = geglu8(st.acc[3], rstd[1]);
    asm volatile("" : "+v"(h0), "+v"(h1));
    put_h(h0, 0, cg + 2);
    put_h(h1, 1, cg + 2);
    if (cg + 2 >= jlo) WAITV(2); else WAITV(0);
    publish(lds, C_KV, 4, ws, WS_FKV + (c * 16 + ti) * FS, tag64, lane);
    __builtin_amdgcn_s_setprio(0);
    if (!isS) {
      ldw(0, 14);
      WAITV(14);
      publish(lds, C_H, 8, ws, WS_FH + (ti * 16 + c) * FS, tag64, lane);
      STAMP(6);
    }
  } else {
    auto w2c = [&](int k) {
      const int G = 26 + (w >> 1) + 2 * k;
      glds16(slot_src(G, wdown, wo, cj) + th * 8192, wl, (unsigned)(size_t)lds + slot_lds(G) + th * 1024);
    };
    put_h(geglu8(st.acc[0], rstd[0]), 0, 2 * cg);
    put_h(geglu8(st.acc[1], rstd[0]), 0, 2 * cg + 1);
    put_h(geglu8(st.acc[2], rstd[1]), 1, 2 * cg);
    put_h(geglu8(st.acc[3], rstd[1]), 1, 2 * cg + 1);
    SB();
    STAMP(5);
    const f32x16 p0 = MFMA(st.wf[0], pack_regs_scaled<0>(st.acc[4], rstd[0]), zero);
    const f32x16 p1 = MFMA(st.wf[1], pack_regs_scaled<8>(st.acc[4], rstd[0]), zero);
    SB();
#pragma unroll
    for (int k = 0; k < 8; ++k) w2c(k);
    bf16x8 ph0[2], ph1[2];
    dual_softmax(p0, ph0);
    asm volatile("" : "+v"(ph0[0]), "+v"(ph0[1]));
    WAITV(8);
    publish(lds, C_H, 8, ws, WS_FH + (ti * 16 + c) * FS, tag64, lane);
    STAMP(6);
    ldw(0, 7);
    dual_softmax(p1, ph1);
    asm volatile("" : "+v"(ph1[0]), "+v"(ph1[1]));
    ldw(7, 14);
    lds_wait_ge(lds, C_XD, 4);
    LAS bf16x8* pq = (LAS bf16x8*)(lds + L_SCR + (2 * th + u0) * 4096 + lane * 16);
    pq[0] = ph0[0]; pq[64] = ph0[1]; pq[128] = ph1[0]; pq[192] = ph1[1];
    WAITL0();
    lds_arrive(lds, C_PQ, lane);
  }
  if (isS) {
    {
      const u64 v = __hip_atomic_load((GAS u64*)(ws + WS_FKV + (c * 16 + (lane & 15)) * FS), RLX_AGENT);
      const bool ok = __all(v == tag64);
      WAITV(0);
      publish(lds, C_H, 8, ws, WS_FH + (ti * 16 + c) * FS, tag64, lane);
      STAMP(6);
      if (!ok) poll16(ws, WS_FKV + c * 16 * FS, 0, 15, tag64, lane);
      __builtin_amdgcn_fence(__ATOMIC_ACQUIRE, "wavefront");
    }
    STAMP(7);
    const unsigned go = WS_KVP + (c * 32 + th) * 2176 + lane * 16;
    u32x4 pv[16][2], pz[2];
#pragma unroll
    for (int q = 0; q < 2; ++q) pz[q] = __builtin_amdgcn_raw_buffer_load_b128(rs, WS_KVP + (c * 32 + th) * 2176 + 2048 + ((lane >> 3) + 8 * q) * 4352 + (lane & 7) * 16, 0, SC1);
#pragma unroll
    for (int t = 0; t < 16; ++t)
#pragma unroll
      for (int q = 0; q < 2; ++q) pv[t][q] = __builtin_amdgcn_raw_buffer_load_b128(rs, go + q * 1024, ((t + 2 * ti) & 15) * 4352, SC1);
    SB();
    lds_arrive(lds, C_G, lane);
    u32x4 ws2[10];
#pragma unroll
    for (int k = 0; k < 10; ++k) ws2[k] = *(const GAS u32x4*)(wo + rot32(22 + k, cj) * 16384 + tg * 8192 + wl);
    SB();
    LAS char* kvf = lds + L_KVF + th * 2048 + ((lane >> 1) & 1) * 1024 + (lane & 1) * 8;
    {
      f32x4 z = __builtin_bit_cast(f32x4, pz[0]) + __builtin_bit_cast(f32x4, pz[1]);
#pragma unroll
      for (int e = 0; e < 4; ++e) {
        float v = dpp_add<0x128>(z[e]);
        auto rr = __builtin_amdgcn_permlane16_swap(asu(v), asu(v), false, false);
        const unsigned r0 = rr[0], r1 = rr[1];
        z[e] = add_x32(asf(r0) + asf(r1));
      }
      *(LAS u32x2*)(kvf + (((lane >> 2) & 1) * 32 + 16) * 16) = u32x2{pk2(z[0], z[1]), pk2(z[2], z[3])};
    }
#pragma unroll
    for (int q = 0; q < 2; ++q) {
      f32x4 a0 = __builtin_bit_cast(f32x4, pv[0][q]);
#pragma unroll
      for (int t = 1; t < 16; ++t) a0 += __builtin_bit_cast(f32x4, pv[t][q]);
      *(LAS u32x2*)(kvf + (q * 32 + (lane >> 2)) * 16) = u32x2{pk2(a0[0], a0[1]), pk2(a0[2], a0[3])};
    }
    WAITL0();
    LAS char* kvr = lds + L_KVF + th * 2048 + (h * 32 + min(r, 16)) * 16;
    const bf16x8 k0 = lds_rd128(kvr), k1 = lds_rd128(kvr + 1024);
    lds_wait_ge(lds, C_PQ, 4);
#pragma unroll
    for (int t = 0; t < 4; ++t) {
      LAS char* pq = lds + L_SCR + (t * 2 + th) * 2048 + lane * 16;
      f32x16 nm = MFMA(k0, lds_rd128(pq), zero);
      nm = MFMA(k1, lds_rd128(pq + 1024), nm);
      const float rd = __builtin_amdgcn_rcpf(nm[8] + 1e-6f);
      __builtin_amdgcn_raw_buffer_store_b128(__builtin_bit_cast(u32x4, pack_regs_scaled<0>(nm, rd)), rs,
                                             WS_O + ((4 * ti + t) * 32 + 2 * c + th) * 1024 + lane * 16, 0, SC1);
    }
    SB();
    lds_arrive(lds, C_R1 + w, lane);
    lds_wait_ge(lds, C_R1 + (w ^ 1), 1);
#pragma unroll
    for (int k = 0; k < 10; ++k) *(LAS u32x4*)(lds + L_SCR + k * 2048 + tg * 1024 + lane * 16) = ws2[k];
    WAITL0();
    lds_arrive(lds, C_R2 + w, lane);
    WAITV(0);
    publish(lds, C_O, 2, ws, WS_FO + (ti * 16 + c) * FS, tag64, lane);
    STAMP(8);
    poll16(ws, WS_FO + (t2 >> 1) * 16 * FS, 0, 15, tag64, lane);
    STAMP(9);
    if (blockIdx.x == 0 && threadIdx.x == 448) *(GAS unsigned*)ws = tag;
    u32x4 a[32];
#pragma unroll
    for (int i = 0; i < 32; ++i) a[i] = __builtin_amdgcn_raw_buffer_load_b128(rs, WS_O + (2 * t2 + tg) * 32 * 1024 + lane * 16, rot32(i, cj) * 1024, SC1);
    SB();
    lds_wait_ge(lds, C_H, 8);
    lds_wait_ge(lds, C_R2 + (w ^ 1), 1);
    steps(IntC<0>{}, IntC<32>{}, a, [&](int i) { return lds + (i < 22 ? slot_lds(45) + i * 2048 : L_SCR + (i - 22) * 2048); });
    lds_wait_ge(lds, C_GD, 6);
    STAMP(11);
    put_part(lds + tg * 30720 + 17408);
    lds_arrive(lds, C_S, lane);
    STAMP(12);
  } else {
    const size_t xoff = ((size_t)t2 * 64 + (threadIdx.x >> 2)) * 1024 + cj * 128 + (threadIdx.x & 3) * 32;
    u32x4 xr[2];
    if (w < 4) { xr[0] = *(const GAS u32x4*)(x + xoff); xr[1] = *(const GAS u32x4*)(x + xoff + 16); }
    poll16(ws, WS_FH + (t2 >> 1) * 16 * FS, kq == 0 ? 0 : (kq == 1 ? 4 : 9), kq == 0 ? 4 : (kq == 1 ? 9 : 14), tag64, lane);
    WAITV(0);
    if (w < 4) lds_arrive(lds, C_W, lane);
    STAMP(8);
    lds_wait_ge(lds, C_G, 2);
    STAMP(9);
    u32x4 a[29];
#pragma unroll
    for (int i = 0; i < 29; ++i) {
      const int j = rot29(i, cj);
      a[i] = __builtin_amdgcn_raw_buffer_load_b128(rs, WS_H + ((2 * t2 + tg) * 86 + st0) * 1024 + lane * 16, (kq == 2 && j == 0) ? 0x40000000 : j * 1024, SC1);
    }
    SB();
    LAS char* img = lds + slot_lds(15 * kq);
    lds_wait_ge(lds, C_H, 8);
    lds_wait_ge(lds, C_W, 4);
    steps(IntC<0>{}, IntC<14>{}, a, [&](int i) { return img + i * 2048; });
    WAITL0();
    lds_arrive(lds, C_R1 + w, lane);
    lds_wait_ge(lds, C_R1 + (w ^ 1), 1);
#pragma unroll
    for (int k = 0; k < 14; ++k) *(LAS u32x4*)(img + k * 2048 + tg * 1024 + lane * 16) = wr[k];
    WAITL0();
    lds_arrive(lds, C_R2 + w, lane);
    lds_wait_ge(lds, C_R2 + (w ^ 1), 1);
    STAMP(10);
    steps(IntC<14>{}, IntC<29>{}, a, [&](int i) { return img + (i % 15) * 2048 + (i == 14 ? 0 : 0); });
    WAITL0();
    lds_arrive(lds, C_R3 + w, lane);
    lds_wait_ge(lds, C_R3 + (w ^ 1), 1);
    put_part(img + tg * 8704);
    lds_arrive(lds, C_GD, lane);
    STAMP(11);
    if (w < 4) {
      lds_wait_ge(lds, C_GD, 6);
      const int ro = (threadIdx.x >> 2) * 272 + (threadIdx.x & 3) * 64;
      float gt[16];
#pragma unroll
      for (int k = 0; k < 4; ++k) {
        const f32x4 gg = *(LAS f32x4*)(lds + ro + k * 16) + *(LAS f32x4*)(lds + 30720 + ro + k * 16) + *(LAS f32x4*)(lds + slot_lds(30) + ro + k * 16);
#pragma unroll
        for (int e = 0; e < 4; ++e) gt[4 * k + e] = gg[e] * __builtin_amdgcn_rcpf(1.f + __builtin_amdgcn_exp2f(-gg[e] * LOG2E));
      }
#pragma unroll
      for (int k = 0; k < 16; k += 4) asm volatile("" : "+v"(gt[k]), "+v"(gt[k + 1]), "+v"(gt[k + 2]), "+v"(gt[k + 3]));
      lds_wait_ge(lds, C_S, 2);
      LAS char* sp = lds + (w >> 1) * 30720 + 17408 + ((threadIdx.x >> 2) & 31) * 272 + (threadIdx.x & 3) * 64;
#pragma unroll
      for (int hf = 0; hf < 2; ++hf) {
        float res[8];
#pragma unroll
        for (int k = 0; k < 2; ++k) {
          const f32x4 ss = *(LAS f32x4*)(sp + hf * 32 + k * 16);
#pragma unroll
          for (int e = 0; e < 4; ++e) {
            const unsigned xw = xr[hf][k * 2 + (e >> 1)];
            res[4 * k + e] = __builtin_fmaf(ss[e], gt[8 * hf + 4 * k + e], asf((e & 1) ? (xw & 0xffff0000u) : (xw << 16)));
          }
        }
        *(GAS bf16x8*)(out + xoff + hf * 16) = pack8(res[0], res[1], res[2], res[3], res[4], res[5], res[6], res[7]);
      }
      STAMP(12);
    }
  }
  DIAG_END();
}

extern "C" void kernel_launch(void* const* d_in, const int* in_sizes, int n_in, void* d_out, int out_size, void* d_ws, size_t ws_size,
                              hipStream_t stream) {
  (void)hipFuncSetAttribute((const void*)fused_block, hipFuncAttributeMaxDynamicSharedMemorySize, LDS_BYTES);
  fused_block<<<256, 512, LDS_BYTES, stream>>>((const char*)d_in[0], (const char*)d_in[1], (const char*)d_in[2], (const char*)d_in[3],
                                               (const char*)d_in[5], (const char*)d_in[6], (const char*)d_in[7], (char*)d_out, (char*)d_ws);
}
```

```cpp
#include <hip/hip_runtime.h>

#define LAS __attribute__((address_space(3)))
#define GAS __attribute__((address_space(1)))
#define DI __device__ __forceinline__
typedef short bf16x8 __attribute__((ext_vector_type(8)));
typedef short s16x4 __attribute__((ext_vector_type(4)));
typedef float f32x16 __attribute__((ext_vector_type(16)));
typedef float f32x4 __attribute__((ext_vector_type(4)));
typedef float f32x2 __attribute__((ext_vector_type(2)));
typedef unsigned u32x4 __attribute__((ext_vector_type(4)));
typedef unsigned u32x2 __attribute__((ext_vector_type(2)));
typedef __bf16 bf2 __attribute__((ext_vector_type(2)));
typedef unsigned long long u64;

constexpr int DM = 512, FF = 1376;
constexpr int XB = 16384, RG = 12288, GOFF = XB, UOFF = XB + RG + 32, QOFF = XB + 2 * RG + 32, STG = XB + 3 * RG + 32;
constexpr int L_WPH = 3 * STG, L_CNT = L_WPH + 1024, L_COL = L_CNT + 256;
constexpr int L_SB = STG, L_SCR = STG + 32768, L_KVF = L_SCR + 16384, L_B2 = 2 * STG;
constexpr int LDS_BYTES = 163840;
constexpr int L_SINK = L_COL + 2048;
static_assert(L_SINK + 256 <= LDS_BYTES && L_KVF + 4096 <= L_B2, "lds");
enum { C_KV = 0, C_H = 1, C_O = 2, C_PQ = 3, C_EX = 4  , C_R1 = 8  , C_R2 = 16  , C_G = 24, C_W = 25, C_P = 26, C_WS = 27 };
constexpr unsigned FS = 256;
constexpr unsigned WS_ST = 4096, WS_CS = 5 * 65536, WS_FKV = 65536, WS_FH = 2 * 65536, WS_FO = 3 * 65536;
constexpr unsigned WS_H = 8 * 65536;
constexpr unsigned WS_O = WS_H + 64u * 86 * 1024;
constexpr unsigned WS_KVP = WS_O + 64u * 32 * 1024;
constexpr unsigned KVB = 2304;
constexpr unsigned WS_END = WS_KVP + 512u * KVB;
constexpr int SC1 = 16;
constexpr int SLOWC = 6;
constexpr unsigned SPIN_G = 1u << 15, SPIN_L = 1u << 19;

#define MFMA(a, b, c) __builtin_amdgcn_mfma_f32_32x32x16_bf16((a), (b), (c), 0, 0, 0)
#define WAITV(n) asm volatile("s_waitcnt vmcnt(" #n ")" ::: "memory")
#define WAITL0() asm volatile("s_waitcnt lgkmcnt(0)" ::: "memory")
#define BAR() __builtin_amdgcn_s_barrier()
#define SB() __builtin_amdgcn_sched_barrier(0)
#define RLX_AGENT __ATOMIC_RELAXED, __HIP_MEMORY_SCOPE_AGENT
#define RLX_WG __ATOMIC_RELAXED, __HIP_MEMORY_SCOPE_WORKGROUP

DI float asf(unsigned u) { return __builtin_bit_cast(float, u); }
DI unsigned asu(float f) { return __builtin_bit_cast(unsigned, f); }
DI unsigned pk2(float a, float b) {
  f32x2 f = {a, b};
  return __builtin_bit_cast(unsigned, __builtin_convertvector(f, bf2));
}
DI bf16x8 pack8(float a0, float a1, float a2, float a3, float a4, float a5, float a6, float a7) {
  u32x4 p = {pk2(a0, a1), pk2(a2, a3), pk2(a4, a5), pk2(a6, a7)};
  return __builtin_bit_cast(bf16x8, p);
}
template <int S0> DI bf16x8 pack_regs(const f32x16& x) {
  return pack8(x[S0], x[S0 + 1], x[S0 + 2], x[S0 + 3], x[S0 + 4], x[S0 + 5], x[S0 + 6], x[S0 + 7]);
}
template <int S0> DI bf16x8 pack_regs_scaled(const f32x16& x, float s) {
  return pack8(x[S0] * s, x[S0 + 1] * s, x[S0 + 2] * s, x[S0 + 3] * s, x[S0 + 4] * s, x[S0 + 5] * s, x[S0 + 6] * s, x[S0 + 7] * s);
}
DI float bf_hi(float v) { return asf(pk2(0.f, v) & 0xffff0000u); }
DI void glds16(const char* sbase, unsigned voff, unsigned ldst) {
  asm volatile("s_mov_b32 m0, %2\n\ts_nop 0\n\tglobal_load_lds_dwordx4 %0, %1" ::"v"(voff), "s"(sbase), "s"(ldst) : "memory");
}
DI void warm64(const char* sbase, unsigned voff, unsigned ldst) {
  asm volatile("s_mov_b32 m0, %2\n\ts_nop 0\n\tglobal_load_lds_dword %0, %1" ::"v"(voff), "s"(sbase), "s"(ldst) : "memory");
}
DI bf16x8 lds_rd128(LAS char* p) { return *(LAS bf16x8*)p; }
DI bf16x8 lds_tr2(LAS char* p0, LAS char* p1) {
  s16x4 lo = __builtin_amdgcn_ds_read_tr16_b64_v4i16((LAS s16x4*)p0);
  s16x4 hi = __builtin_amdgcn_ds_read_tr16_b64_v4i16((LAS s16x4*)p1);
  return __builtin_shufflevector(lo, hi, 0, 1, 2, 3, 4, 5, 6, 7);
}
#define MX(a, b) __builtin_elementwise_maximum((a), (b))
DI float max_x32(float v) {
  auto r = __builtin_amdgcn_permlane32_swap(asu(v), asu(v), false, false);
  const unsigned r0 = r[0], r1 = r[1];
  return MX(asf(r0), asf(r1));
}
DI float add_x32(float v) {
  auto r = __builtin_amdgcn_permlane32_swap(asu(v), asu(v), false, false);
  const unsigned r0 = r[0], r1 = r[1];
  return asf(r0) + asf(r1);
}
template <int CTRL> DI float dpp_add(float v) { return v + asf(__builtin_amdgcn_update_dpp(0u, asu(v), CTRL, 0xf, 0xf, true)); }
constexpr float LOG2E = 1.4426950408889634f;

DI void dual_softmax(const f32x16& p, bf16x8 (&o)[2]) {
  float m[2];
#pragma unroll
  for (int g = 0; g < 2; ++g)
    m[g] = MX(MX(MX(MX(p[8 * g], p[8 * g + 1]), p[8 * g + 2]), MX(MX(p[8 * g + 3], p[8 * g + 4]), p[8 * g + 5])), MX(p[8 * g + 6], p[8 * g + 7]));
  f32x2 e[2][4];
#pragma unroll
  for (int g = 0; g < 2; ++g) {
    const float mm = max_x32(m[g]) * -LOG2E;
#pragma unroll
    for (int i = 0; i < 4; ++i) {
      const f32x2 t = __builtin_elementwise_fma(f32x2{p[8 * g + 2 * i], p[8 * g + 2 * i + 1]}, f32x2{LOG2E, LOG2E}, f32x2{mm, mm});
      e[g][i] = f32x2{__builtin_amdgcn_exp2f(t[0]), __builtin_amdgcn_exp2f(t[1])};
    }
  }
#pragma unroll
  for (int g = 0; g < 2; ++g) {
    const f32x2 s2 = (e[g][0] + e[g][1]) + (e[g][2] + e[g][3]);
    const float rc = __builtin_amdgcn_rcpf(add_x32(s2[0] + s2[1]));
    u32x4 q;
#pragma unroll
    for (int i = 0; i < 4; ++i) q[i] = __builtin_bit_cast(unsigned, __builtin_convertvector(e[g][i] * rc, bf2));
    o[g] = __builtin_bit_cast(bf16x8, q);
  }
}
DI u32x4 geglu8(const f32x16& acc, float rs) {
  const float c1 = -2.f * 0.7978845608028654f * LOG2E, c2 = c1 * 0.044715f;
  const float k1 = c1 * rs, rs2 = rs * rs, k2 = c2 * rs2 * rs, ir2 = __builtin_amdgcn_rcpf(rs2);
  u32x4 o;
#pragma unroll
  for (int i = 0; i < 4; ++i) {
    const f32x2 a = {acc[2 * i], acc[2 * i + 1]}, b = {acc[8 + 2 * i], acc[9 + 2 * i]};
    const f32x2 t = a * __builtin_elementwise_fma(a * a, f32x2{k2, k2}, f32x2{k1, k1});
    const f32x2 ex = {__builtin_amdgcn_exp2f(t[0]), __builtin_amdgcn_exp2f(t[1])};
    const f32x2 d = __builtin_elementwise_fma(ex, f32x2{ir2, ir2}, f32x2{ir2, ir2});
    const f32x2 rc = {__builtin_amdgcn_rcpf(d[0]), __builtin_amdgcn_rcpf(d[1])};
    o[i] = __builtin_bit_cast(unsigned, __builtin_convertvector((a * b) * rc, bf2));
  }
  return o;
}
DI void lds_wait_ge(LAS char* lds, int idx, unsigned want) {
  volatile LAS unsigned* p = (volatile LAS unsigned*)(lds + L_CNT) + idx;
#pragma unroll 1
  for (unsigned s = 0; s < SPIN_L; ++s) {
    if (__builtin_amdgcn_readfirstlane(*p) >= want) break;
    __builtin_amdgcn_s_sleep(1);
  }
  asm volatile("" ::: "memory");
}
DI unsigned lds_arrive(LAS char* lds, int idx, int lane) {
  unsigned old = 0;
  if (lane == 0) old = __hip_atomic_fetch_add((LAS unsigned*)(lds + L_CNT) + idx, 1u, RLX_WG);
  return __builtin_amdgcn_readfirstlane(old);
}
DI void lds_mark(LAS char* lds, int idx) {
  asm volatile("s_mov_b64 exec, 1\n\tds_add_u32 %0, %1\n\ts_mov_b64 exec, -1" ::"v"((unsigned)(size_t)lds + L_CNT + 4 * idx), "v"(1u) : "memory");
}
DI void publish(LAS char* lds, int idx, unsigned n, char* ws, unsigned flag_off, u64 tag64, int lane) {
  if (lds_arrive(lds, idx, lane) == n - 1 && lane < 16) __hip_atomic_store((GAS u64*)(ws + flag_off) + lane, tag64, RLX_AGENT);
}
DI void poll16(const char* ws, unsigned off, int lo, int hi, u64 tag64, int lane) {
  const int l = min(max(lane & 15, lo), hi);
  GAS u64* p = (GAS u64*)(ws + off + l * FS);
#pragma unroll 1
  for (unsigned s = 0; s < SPIN_G; ++s) {
    const u64 v = __hip_atomic_load(p, RLX_AGENT);
    if (__all(v == tag64)) break;
    __builtin_amdgcn_s_sleep(2);
  }
  __builtin_amdgcn_fence(__ATOMIC_ACQUIRE, "wavefront");
}

template <bool V> struct BoolC { static constexpr bool value = V; };
template <int V> struct IntC { static constexpr int value = V; };
struct Frags { bf16x8 x[2]; bf16x8 w[3]; };
struct Pro { unsigned xo[2], go[2], qo[2]; };
struct P1State { f32x16 acc[5]; float rstd[2]; bf16x8 wf[2], idf[2]; unsigned tag; };

DI const char* dstep(const char* wdown, int p, int s) { return wdown + (size_t)(30 * p + s) * 16384; }
constexpr int s_lds(int s) { return s < 2 ? 49152 + s * 2048 : (s < 6 ? L_B2 + 43008 + (s - 2) * 2048 : L_SB + (s - 6) * 2048); }

template <bool LO>
DI void p1_mainloop(P1State& st, const Pro& pr, LAS char* lds, const char* xg, const char* wG, const char* wU, const char* wQ, const char* wdown, const char* wo,
                    char* ws, int ti, int c, unsigned wl, int w, int lane, int e, int xcd, const char* wg0, const char* wu0, const char* wq0, int f0, int fw) {
  const int th = w & 1, cg = w >> 1, r = lane & 31, h = lane >> 5;
  const unsigned lb = (unsigned)(size_t)lds;
  const unsigned(&xo)[2] = pr.xo, (&go)[2] = pr.go, (&qo)[2] = pr.qo;
  const int p2 = 8 + (w & 3);
  const int gst = fw < 15 ? 128 * FF : 0;
  auto piece = [&](int k, int s, unsigned bo) {
    const unsigned b = lb + bo;
    if (k == 0) glds16(xg + s * 128, xo[0], b + 2 * w * 1024);
    if (k == 1) glds16(wG + s * gst, go[0], b + GOFF + w * 1024);
    if (k == 2) glds16(wU + s * gst, go[0], b + UOFF + w * 1024);
    if (k == 3) glds16(wQ + s * (64 * 3072), qo[0], b + QOFF + w * 1024);
    if (k == 4) glds16(xg + s * 128, xo[1], b + (2 * w + 1) * 1024);
    if (k == 5) glds16((LO ? wG : wU) + s * gst, go[1], b + (LO ? GOFF : UOFF) + p2 * 1024);
    if (k == 6 && !LO) glds16(wQ + s * (64 * 3072), qo[1], b + QOFF + p2 * 1024);
  };
  const char *w2d = wdown + cg * 16384 + th * 8192, *w2o = wo + cg * 16384 + th * 8192;
  auto w2piece = [&](int k, bool first) {
    const unsigned dst = lb + (first ? L_B2 : 0) + (cg + 4 * k) * 2048 + th * 1024;
    const char* sd = w2d + (30 * (k >> 1) + 4 * (k & 1) + (first ? 8 : 0)) * 16384;
    if (k < (first ? 5 : 6)) glds16(sd, wl, dst);
    else if (k == 5) glds16(cg == 0 ? sd : w2o + 16384, wl, dst);
    else if (cg < (first ? 1 : 2)) glds16(w2o + (first ? 5 * 16384 : 0), wl, dst);
  };
  u32x4 xrow;
  unsigned cw[11];
  const int myrow = 8 * c + w;
  const int el = min((xcd & 1) * 32 + (e >> 3), 53), cl = el / 3, part = el - 3 * cl, cbx = xcd >> 1;
  const int ch = cl < 6 ? (cl >> 1) * 8 + 2 * cbx + (cl & 1) : (cl < 12 ? min(18 + 6 * cbx + cl, 45) : min(34 + 6 * cbx + cl, 67));
  const unsigned astr = ch < 24 ? 3072 : 2 * FF;
  const char* abase = (ch < 24 ? wq0 + ch * 128 : (ch < 46 ? wg0 + (ch - 24) * 128 : wu0 + (ch - 46) * 128)) + (size_t)(176 * part + (part < 2 ? 22 : 20) * w) * astr;
  const unsigned avo = h * astr + ((ch == 45 || ch == 67) ? (r & 15) : r) * 4;
  auto aux_load = [&](int t) {
    if (t == 11) asm volatile("global_load_dwordx4 %0, %1, %2" : "=v"(xrow) : "v"(myrow * 1024 + lane * 16), "s"(xg) : "memory");
    else asm volatile("global_load_dword %0, %1, %2" : "=v"(cw[t]) : "v"(avo), "s"(abase + (size_t)(2 * (t < 10 || part < 2 ? t : 9)) * astr) : "memory");
  };
  unsigned tag;
  asm volatile("s_load_dword %0, %1, 0x0" : "=s"(tag) : "s"(ws) : "memory");
  const int g = (lane >> 4) & 1, u0 = LO ? cg : 0;
  const int wrow = (8 * h + ((lane & 15) >> 2)) * 192 + (lane & 3) * 8;
  const int wFF = (g ? UOFF : GOFF) + wrow + (LO ? 64 * cg : 32 * (cg + 2));
  const int wQK = QOFF + wrow + g * 32 + (LO ? 128 : 64 * (cg - 2));
  int xad[4];
#pragma unroll
  for (int kk = 0; kk < 4; ++kk) xad[kk] = (64 * th + r) * 128 + (((2 * kk + h) ^ ((r >> 1) & 7)) << 4);
  const int xa0 = u0 * 4096, xa1 = 4096 - xa0;
  auto load_part = [&](Frags& f, int bo, int kk, int part) {
    LAS char* xa = lds + bo + xad[kk];
    LAS char* a = lds + bo + wFF + kk * 3072;
    LAS char* q = lds + bo + wQK + kk * 3072;
    if (part == 0) { f.x[0] = lds_rd128(xa + xa0); f.w[0] = LO ? lds_tr2(a, a + 768) : lds_tr2(q, q + 768); }
    if (part == 1) f.w[1] = LO ? lds_tr2(a + 32, a + 32 + 768) : lds_tr2(a, a + 768);
    if (part == 2) f.x[1] = lds_rd128(xa + xa1);
    if (part == 3 && LO) f.w[2] = lds_tr2(q, q + 768);
  };
#pragma unroll
  for (int i = 0; i < 5; ++i)
#pragma unroll
    for (int j = 0; j < 16; ++j) st.acc[i][j] = (!LO && i == 4 && r >= 16) ? 1.f : 0.f;
  if (!LO) {
#pragma unroll
    for (int s = 0; s < 2; ++s)
#pragma unroll
      for (int j = 0; j < 8; ++j) st.idf[s][j] = (16 * s + 8 * (j >> 2) + 4 * h + (j & 3) == r) ? (short)0x3f80 : (short)0;
    asm volatile("" : "+v"(st.idf[0]), "+v"(st.idf[1]), "+v"(st.acc[4]));
  }
  asm volatile("s_waitcnt lgkmcnt(0)" : "+s"(tag)::"memory");
  tag += 1u;
  st.tag = tag;
  float av[8], amean, avar, s0 = 0.f, s1 = 0.f;
  auto aux_work = [&](int ph) {
    if (ph < 11) {
      const unsigned t = (ph == 10 && part == 2) ? 0u : cw[ph];
      s0 += asf(t << 16); s1 += asf(t & 0xffff0000u);
      asm volatile("" : "+v"(s0), "+v"(s1));
    }
    if (ph == 11) *(LAS f32x2*)(lds + L_COL + w * 256 + r * 8) = f32x2{add_x32(s0), add_x32(s1)};
    if (ph == 0) {
#pragma unroll
      for (int i = 0; i < 4; ++i) { const unsigned t = xrow[i]; av[2 * i] = asf(t << 16); av[2 * i + 1] = asf(t & 0xffff0000u); }
    }
    if (ph == 1) amean = ((av[0] + av[1]) + (av[2] + av[3])) + ((av[4] + av[5]) + (av[6] + av[7]));
    if (ph == 2 || ph == 8) { float& v = ph == 2 ? amean : avar; v = dpp_add<0x4E>(dpp_add<0xB1>(v)); }
    if (ph == 3 || ph == 9) { float& v = ph == 3 ? amean : avar; v = dpp_add<0x140>(dpp_add<0x141>(v)); }
    if (ph == 4 || ph == 10) {
      float& v = ph == 4 ? amean : avar;
      auto rr = __builtin_amdgcn_permlane16_swap(asu(v), asu(v), false, false);
      const unsigned r0 = rr[0], r1 = rr[1];
      v = asf(r0) + asf(r1);
    }
    if (ph == 5) amean = add_x32(amean) * (1.f / DM);
    if (ph == 6) { avar = 0.f;
#pragma unroll
      for (int i = 0; i < 4; ++i) { const float d = av[i] - amean; avar = __builtin_fmaf(d, d, avar); } }
    if (ph == 7) {
#pragma unroll
      for (int i = 4; i < 8; ++i) { const float d = av[i] - amean; avar = __builtin_fmaf(d, d, avar); } }
    if (ph == 12) avar = __builtin_amdgcn_rsqf(add_x32(avar) * (1.f / DM) + 1e-5f);
    if (ph == 13) {
      const u32x4 gr = {asu(amean), tag, asu(avar), tag};
      asm volatile("s_mov_b64 exec, 1\n\tglobal_store_dwordx4 %0, %1, %2 sc1\n\ts_mov_b64 exec, -1\n\ts_nop 0" ::"v"(WS_ST + (ti * 128 + myrow) * 16), "v"(gr), "s"(ws) : "memory");
    }
    if (ph >= 1 && ph <= 5) asm volatile("" : "+v"(amean));
    if (ph >= 6 && ph <= 12) asm volatile("" : "+v"(avar));
  };
  Frags f[2];
  u32x4 stt[2];
  u32x2 csg[3][2];
  unsigned cso[3];
  cso[LO ? 0 : 1] = 1536 + (r >> 4) * 1408 + f0 + (LO ? 32 * cg : 16 * (cg + 2)) + (r & 15);
  cso[LO ? 1 : 0] = LO ? cso[0] + 16 : 512 + (r >> 4) * 512 + 32 * c + 16 * (cg - 2) + (r & 15);
  cso[2] = 32 * c + r;
  auto mma = [&](const Frags& cf, auto&& gap, auto&& rd, auto&& dma0, auto&& dma1) {
    SB();
    st.acc[0] = MFMA(cf.w[0], cf.x[0], st.acc[0]); SB(); rd(0); gap(0); SB();
    st.acc[1] = MFMA(cf.w[1], cf.x[0], st.acc[1]); SB(); rd(1); dma0(); gap(1); SB();
    st.acc[2] = MFMA(cf.w[0], cf.x[1], st.acc[2]); SB(); rd(2); gap(2); SB();
    st.acc[3] = MFMA(cf.w[1], cf.x[1], st.acc[3]); SB(); rd(3); dma1(); gap(3); SB();
    if (LO) { st.acc[4] = MFMA(cf.w[2], cf.x[0], st.acc[4]); SB(); }
  };
  auto wfrag = [&](int hh) {
    LAS char* p = lds + L_WPH + hh * 512 + (4 * h + ((lane & 15) >> 2)) * 32 + (lane & 3) * 8;
    s16x4 lo = __builtin_amdgcn_ds_read_tr16_b64_v4i16((LAS s16x4*)p);
    s16x4 hi = __builtin_amdgcn_ds_read_tr16_b64_v4i16((LAS s16x4*)(p + 256));
    const unsigned neg = (lane & 16) ? 0x80008000u : 0u;
    u32x2 a = __builtin_bit_cast(u32x2, lo), b = __builtin_bit_cast(u32x2, hi);
    return __builtin_bit_cast(bf16x8, u32x4{a[0] ^ neg, a[1] ^ neg, b[0] ^ neg, b[1] ^ neg});
  };
  int cur = 0;
  auto iter = [&](int s, auto mode_tag) {
    constexpr int MODE = decltype(mode_tag)::value;
    const int fre = (cur == 0) ? 2 * STG : cur - STG;
    auto dma = [&](int k) {
      if (MODE <= 1) piece(k, s + 2, fre);
      if (MODE == 2) w2piece(k, true);
      if (MODE == 3) w2piece(k, false);
    };
    WAITL0();
    if (MODE == 3) WAITV(12);
    else if (MODE == -2) WAITV(18);
    else if (MODE == -1) asm volatile("s_waitcnt vmcnt(6)" : "+v"(xrow), "+v"(cw[0]), "+v"(cw[1]), "+v"(cw[2]), "+v"(cw[3]), "+v"(cw[4]), "+v"(cw[5]), "+v"(cw[6]), "+v"(cw[7]), "+v"(cw[8]), "+v"(cw[9]), "+v"(cw[10])::"memory");
    else WAITV(6);
    BAR();
    if (MODE == -2 && LO && w == 0) warm64(blockIdx.x < 172 ? wdown + blockIdx.x * 8192 : wo + min((int)blockIdx.x - 172, 63) * 8192, lane * 128, lb + L_SINK);
    float cpv[8];
    const bool pub = MODE == 1 && !LO && s == 3 && w == 7;
    auto gap = [&](int gi) {
      if (MODE == 0 && gi >= 4) aux_load(gi - 4);
      if (MODE == -1) aux_work(gi);
      if (MODE == 1 && !LO && gi == 0 && pub) {
#pragma unroll
        for (int i = 0; i < 8; ++i) cpv[i] = ((LAS float*)(lds + L_COL))[64 * i + lane];
      }
      if (MODE == 1 && !LO && gi == 6 && pub) {
        const float v = ((cpv[0] + cpv[1]) + (cpv[2] + cpv[3])) + ((cpv[4] + cpv[5]) + (cpv[6] + cpv[7]));
        asm volatile("global_store_dwordx2 %0, %1, %2 sc1" ::"v"(WS_CS + (part * 4352 + ch * 64 + lane) * 8), "v"(u32x2{asu(v), tag}), "s"(ws) : "memory");
      }
      if (MODE == 2 && gi < 2)
        asm volatile("global_load_dwordx4 %0, %1, %2 sc1" : "=v"(stt[gi]) : "v"(WS_ST + (ti * 128 + 64 * th + 32 * (gi ^ u0) + r) * 16), "s"(ws) : "memory");
      if (MODE == 2 && gi >= 2 && gi < (LO ? 8 : 6))
        asm volatile("global_load_dwordx2 %0, %1, %2 sc1" : "=v"(csg[(gi - 2) >> 1][gi & 1]) : "v"(WS_CS + (((gi & 1) ? 2 : h) * 4352 + cso[(gi - 2) >> 1]) * 8), "s"(ws) : "memory");
    };
    if (MODE != 0) mma(f[1], [&](int g) { gap(g); }, [&](int p) { load_part(f[0], cur, 0, p); }, [&] { dma(0); }, [&] { dma(1); });
    else { load_part(f[0], cur, 0, 0); load_part(f[0], cur, 0, 1); load_part(f[0], cur, 0, 2); load_part(f[0], cur, 0, 3); dma(0); dma(1); }
    mma(f[0], [&](int g) { gap(4 + g); }, [&](int p) { load_part(f[1], cur, 1, p); }, [&] { dma(2); }, [&] { dma(3); });
    if (MODE == 3) {
      asm volatile("s_waitcnt vmcnt(6)" : "+v"(stt[0]), "+v"(stt[1]), "+v"(csg[0][0]), "+v"(csg[0][1]), "+v"(csg[1][0]), "+v"(csg[1][1])::"memory");
      if (LO) asm volatile("" : "+v"(csg[2][0]), "+v"(csg[2][1]));
#pragma unroll 1
      for (unsigned sp = 0; sp < SPIN_G; ++sp) {
        unsigned lo = min(min(stt[0][1], stt[0][3]), min(stt[1][1], stt[1][3])), hi = max(max(stt[0][1], stt[0][3]), max(stt[1][1], stt[1][3]));
#pragma unroll
        for (int i = 0; i < (LO ? 3 : 2); ++i) { lo = min(lo, min(csg[i][0][1], csg[i][1][1])); hi = max(hi, max(csg[i][0][1], csg[i][1][1])); }
        unsigned bad = (lo ^ tag) | (hi ^ tag);
        asm volatile("" : "+v"(bad));
        if (__all(bad == 0u)) break;
#pragma unroll
        for (int u = 0; u < 2; ++u)
          asm volatile("global_load_dwordx4 %0, %1, %2 sc1\n\ts_waitcnt vmcnt(0)" : "=&v"(stt[u]) : "v"(WS_ST + (ti * 128 + 64 * th + 32 * (u ^ u0) + r) * 16), "s"(ws) : "memory");
#pragma unroll
        for (int i = 0; i < (LO ? 3 : 2); ++i)
#pragma unroll
          for (int q = 0; q < 2; ++q)
            asm volatile("global_load_dwordx2 %0, %1, %2 sc1\n\ts_waitcnt vmcnt(0)" : "=&v"(csg[i][q]) : "v"(WS_CS + ((q ? 2 : h) * 4352 + cso[i]) * 8), "s"(ws) : "memory");
      }
      bf16x8 mf[2], cf[3];
#pragma unroll
      for (int u = 0; u < 2; ++u) {
        const unsigned mu = stt[u][0], ru = stt[u][2];
        const float mean = -asf(mu), mh = bf_hi(mean);
        st.rstd[u] = asf(ru);
        mf[u] = __builtin_bit_cast(bf16x8, u32x4{pk2(mh, mean - mh), pk2(mh, 0.f), 0u, 0u});
      }
#pragma unroll
      for (int i = 0; i < (LO ? 3 : 2); ++i) {
        const unsigned a0 = csg[i][0][0], a1 = csg[i][1][0];
        const float cs = asf(a0) + (h ? 0.f : asf(a1)), chi = bf_hi(cs);
        cf[i] = __builtin_bit_cast(bf16x8, u32x4{pk2(chi, chi), pk2(cs - chi, 0.f), 0u, 0u});
      }
      st.acc[0] = MFMA(cf[0], mf[0], st.acc[0]);
      st.acc[1] = MFMA(cf[1], mf[0], st.acc[1]);
      st.acc[2] = MFMA(cf[0], mf[1], st.acc[2]);
      st.acc[3] = MFMA(cf[1], mf[1], st.acc[3]);
      if (LO) st.acc[4] = MFMA(cf[2], mf[0], st.acc[4]);
      st.wf[0] = wfrag(LO ? 0 : cg - 2);
      if (LO) st.wf[1] = wfrag(1);
      asm volatile("" : "+v"(st.wf[0]));
      if (LO) asm volatile("" : "+v"(st.wf[1]));
    }
    mma(f[1], [&](int g) { gap(8 + g); }, [&](int p) { load_part(f[0], cur, 2, p); }, [&] { dma(4); }, [&] { dma(5); });
    mma(f[0], [&](int g) { gap(12 + g); }, [&](int p) { load_part(f[1], cur, 3, p); }, [&] { dma(6); }, [] {});
    cur = (cur == 2 * STG) ? 0 : cur + STG;
  };
  iter(0, IntC<0>{});
  iter(1, IntC<-2>{});
  iter(2, IntC<-1>{});
#pragma unroll 1
  for (int s = 3; s < 6; ++s) iter(s, IntC<1>{});
  iter(6, IntC<2>{});
  iter(7, IntC<3>{});
  mma(f[1], [](int) {}, [](int) {}, [] {}, [] {});
  WAITL0();
  BAR();
}

template <bool LO>
DI void run_wave(const char* x, const char* wqkv, const char* wo, const char* wgate, const char* wup, const char* wdown, char* out,
                 char* ws, LAS char* lds, int w, const Pro& pr) {
  const int lane = threadIdx.x & 63;
  const int xcd = blockIdx.x & 7, idx = blockIdx.x >> 3;
  const int ti = 8 * (xcd & 1) + (idx & 7), c = 4 * (xcd >> 1) + (idx >> 3);
  const int t2 = 4 * xcd + (idx & 3), cj = idx >> 2;
  const int th = w & 1, cg = w >> 1, r = lane & 31, h = lane >> 5;
  const int fw = c < SLOWC ? c : (c < SLOWC + 2 ? c - SLOWC + 14 : c - 2);
  const int f0 = min(96 * fw, FF - 96);
  const char* xg = x + (size_t)ti * 128 * 1024;
  const unsigned wl = (lane >> 3) * 1024 + cj * 128 + (((lane & 7) ^ (((lane >> 4) & 1) << 2)) << 4);
  const __amdgpu_buffer_rsrc_t rs = __builtin_amdgcn_make_buffer_rsrc((void*)ws, 0, (int)WS_END, 0x00020000);
  P1State st;
  p1_mainloop<LO>(st, pr, lds, xg, wgate + 2 * f0, wup + 2 * f0, wqkv + c * 64, wdown, wo, ws, ti, c, wl, w, lane, idx * 8 + w, xcd, wgate, wup, wqkv, f0, fw);

  const unsigned tag = st.tag;
  const u64 tag64 = ((u64)tag << 32) | (u64)(~tag);
  const int u0 = LO ? cg : 0;
  const float(&rstd)[2] = st.rstd;
  f32x16 zero;
#pragma unroll
  for (int j = 0; j < 16; ++j) zero[j] = 0.f;
  const int jlo = fw < 14 ? 0 : (fw == 14 ? 4 : 6);
  auto put_h = [&](const u32x4& o, int u, int j) {
    if (j >= jlo) __builtin_amdgcn_raw_buffer_store_b128(o, rs, WS_H + ((4 * ti + 2 * th + (u ^ u0)) * 86 + (f0 >> 4) + j) * 1024 + lane * 16, 0, SC1);
  };
  const int tg = th, kq = cg;
  const bool isS = kq == 3;
  const int q4 = (lane & 15) >> 2, p4 = lane & 3, g4 = (lane >> 4) & 1;
  int tro[2];
#pragma unroll
  for (int cc = 0; cc < 2; ++cc) tro[cc] = (4 * h + q4) * 128 + (((4 * cc + 2 * g4 + (p4 >> 1)) ^ ((q4 >> 1) << 2)) << 4) + (p4 & 1) * 8;
  f32x16 acc[2] = {zero, zero};
  const unsigned lb = (unsigned)(size_t)lds;
  auto rdw = [&](bf16x8 (&d)[2], LAS char* s) { d[0] = lds_tr2(s + tro[0], s + tro[0] + 1024); d[1] = lds_tr2(s + tro[1], s + tro[1] + 1024); };
  auto put_part = [&](LAS char* tile) {
#pragma unroll
    for (int cc = 0; cc < 2; ++cc)
#pragma unroll
      for (int k = 0; k < 4; ++k)
        *(LAS f32x4*)(tile + r * 256 + (((8 * cc + 2 * k + h) ^ (r & 15)) << 4)) = f32x4{acc[cc][4 * k], acc[cc][4 * k + 1], acc[cc][4 * k + 2], acc[cc][4 * k + 3]};
    WAITL0();
  };
  LAS char *sa = lds + kq * 16384, *sb = lds + L_B2 + kq * 16384;
  auto slot = [&](int s) { return s < 8 ? sa + s * 2048 : sb + (s - 8) * 2048; };
  auto d_pre = [&](auto m_tag, bf16x8 (*pre)[2]) {
    lds_wait_ge(lds, C_P, 8);
#pragma unroll
    for (int k = 0; k < decltype(m_tag)::value; ++k) rdw(pre[k], slot(k));
    WAITL0();
    lds_mark(lds, C_R1 + w);
  };
  auto d_refill = [&](int R, int k0, int k1) {
    SB();
#pragma unroll
    for (int k = k0; k < k1; ++k) glds16(dstep(wdown, kq, R + k) + tg * 8192, wl, (unsigned)(size_t)slot(k) + tg * 1024);
    SB();
  };
  auto d_phase = [&](auto n_tag, auto m_tag, auto fin_tag, const bf16x8 (*pre)[2]) {
    constexpr int N = decltype(n_tag)::value, M = decltype(m_tag)::value, R = N - M, PD = 3, NA = 12;
    constexpr bool FIN = decltype(fin_tag)::value;
    const size_t xoff = ((size_t)t2 * 64 + (threadIdx.x >> 2)) * 1024 + cj * 128 + (threadIdx.x & 3) * 32;
    u32x4 xr[2];
    if (FIN) { xr[0] = *(const GAS u32x4*)(x + xoff); xr[1] = *(const GAS u32x4*)(x + xoff + 16); }
    poll16(ws, WS_FH + (t2 >> 1) * 16 * FS, 5 * kq, 5 * kq + 4, tag64, lane);
    WAITV(0);
    lds_mark(lds, C_R2 + w);
    if (FIN) lds_mark(lds, C_W);
    lds_wait_ge(lds, C_G, 2);
    u64 oflag;
    {
      const unsigned aoff = WS_H + ((2 * t2 + tg) * 86 + 30 * kq) * 1024 + lane * 16;
      u32x4 a[N];
      bf16x8 wf[PD + 1][2];
#pragma unroll
      for (int i = 0; i < NA; ++i) a[i] = __builtin_amdgcn_raw_buffer_load_b128(rs, aoff + i * 1024, 0, SC1);
      SB();
#pragma unroll
      for (int i = 0; i < N; ++i) {
        const bf16x8 av = __builtin_bit_cast(bf16x8, a[i]);
        acc[0] = MFMA(i < M ? pre[i][0] : wf[i % (PD + 1)][0], av, acc[0]);
        SB();
#pragma unroll
        for (int k = 0; k < 2; ++k) if (NA + 2 * i + k < N) a[NA + 2 * i + k] = __builtin_amdgcn_raw_buffer_load_b128(rs, aoff + (NA + 2 * i + k) * 1024, 0, SC1);
        if (i + PD == R) lds_wait_ge(lds, C_R2 + (w ^ 1), 1);
        if (FIN && i == N - 8) oflag = __hip_atomic_load((GAS u64*)(ws + WS_FO + ((t2 >> 1) * 16 + 8 * kq + (lane & 7)) * FS), RLX_AGENT);
        if (i + PD >= M && i + PD < N) rdw(wf[(i + PD) % (PD + 1)], slot((i + PD) % R));
        SB();
        acc[1] = MFMA(i < M ? pre[i][1] : wf[i % (PD + 1)][1], av, acc[1]);
        SB();
      }
    }
    WAITL0();
    lds_mark(lds, C_R1 + w);
    lds_wait_ge(lds, C_R1 + (w ^ 1), 2);
    put_part(tg ? sb : sa);
    if constexpr (FIN) {
      if (!__all(oflag == tag64)) poll16(ws, WS_FO + (t2 >> 1) * 16 * FS, 8 * kq, 8 * kq + 7, tag64, lane);
      __builtin_amdgcn_fence(__ATOMIC_ACQUIRE, "wavefront");
      u32x4 ao[16];
#pragma unroll
      for (int i = 0; i < 16; ++i) ao[i] = __builtin_amdgcn_raw_buffer_load_b128(rs, WS_O + ((2 * t2 + tg) * 32 + 16 * kq + i) * 1024 + lane * 16, 0, SC1);
      SB();
      bf16x8 wq[16][2];
      lds_wait_ge(lds, C_W, 4);
      if (kq == 1) lds_wait_ge(lds, C_WS, 2);
#pragma unroll
      for (int i = 0; i < 16; ++i) rdw(wq[i], lds + (kq == 0 ? s_lds(i) : (i < 6 ? s_lds(16 + i) : L_SCR + (i - 6) * 2048)));
      acc[0] = zero; acc[1] = zero;
#pragma unroll
      for (int i = 0; i < 16; ++i) {
        acc[0] = MFMA(wq[i][0], __builtin_bit_cast(bf16x8, ao[i]), acc[0]);
        acc[1] = MFMA(wq[i][1], __builtin_bit_cast(bf16x8, ao[i]), acc[1]);
      }
      put_part((tg ? sb : sa) + 8192);
      BAR();
      asm volatile("" ::: "memory");
      const int tk = (threadIdx.x >> 2) & 31;
      LAS char* rb = lds + ((w >> 1) ? L_B2 : 0) + tk * 256;
      auto rd = [&](int off, int k) { return *(LAS f32x4*)(rb + off + (((4 * (threadIdx.x & 3) + k) ^ (tk & 15)) << 4)); };
      if (blockIdx.x == 0 && threadIdx.x == 0) __hip_atomic_store((GAS unsigned*)ws, tag, RLX_AGENT);
#pragma unroll
      for (int hf = 0; hf < 2; ++hf) {
        float res[8];
#pragma unroll
        for (int k = 0; k < 2; ++k) {
          const f32x4 ss = rd(8192, 2 * hf + k) + rd(16384 + 8192, 2 * hf + k);
          const f32x4 gg = rd(0, 2 * hf + k) + rd(16384, 2 * hf + k) + rd(32768, 2 * hf + k);
#pragma unroll
          for (int e = 0; e < 4; ++e) {
            const unsigned xw = xr[hf][k * 2 + (e >> 1)];
            res[4 * k + e] = __builtin_fmaf(ss[e] * gg[e], __builtin_amdgcn_rcpf(1.f + __builtin_amdgcn_exp2f(-gg[e] * LOG2E)), asf((e & 1) ? (xw & 0xffff0000u) : (xw << 16)));
          }
        }
        *(GAS bf16x8*)(out + xoff + hf * 16) = pack8(res[0], res[1], res[2], res[3], res[4], res[5], res[6], res[7]);
      }
    }
  };
  if constexpr (!LO) {
    __builtin_amdgcn_s_setprio(2);
    const int hh = cg - 2;
    const bf16x8(&idf)[2] = st.idf;
    f32x16 p0 = MFMA(st.wf[0], pack_regs_scaled<0>(st.acc[0], rstd[0]), zero);
    f32x16 p1 = MFMA(st.wf[0], pack_regs_scaled<0>(st.acc[2], rstd[1]), zero);
    const f32x16 vt0 = MFMA(pack_regs_scaled<8>(st.acc[0], rstd[0]), idf[0], st.acc[4]);
    const f32x16 vt1 = MFMA(pack_regs_scaled<8>(st.acc[2], rstd[1]), idf[0], st.acc[4]);
    bf16x8 ph0[2], ph1[2];
    dual_softmax(p0, ph0);
    f32x16 pt0 = MFMA(ph0[0], idf[0], zero);
    pt0 = MFMA(ph0[1], idf[1], pt0);
    dual_softmax(p1, ph1);
    f32x16 pt1 = MFMA(ph1[0], idf[0], zero);
    pt1 = MFMA(ph1[1], idf[1], pt1);
    f32x16 kv = MFMA(pack_regs<0>(pt0), pack_regs<0>(vt0), zero);
    kv = MFMA(pack_regs<8>(pt0), pack_regs<8>(vt0), kv);
    kv = MFMA(pack_regs<0>(pt1), pack_regs<0>(vt1), kv);
    kv = MFMA(pack_regs<8>(pt1), pack_regs<8>(vt1), kv);
    WAITV(0);
    lds_mark(lds, C_P);
    LAS f32x4* ex = (LAS f32x4*)(lds + L_KVF);
    f32x4 kq4[4];
#pragma unroll
    for (int q = 0; q < 4; ++q) {
      kq4[q] = f32x4{kv[4 * q], kv[4 * q + 1], kv[4 * q + 2], kv[4 * q + 3]};
      asm volatile("" : "+v"(kq4[q]));
    }
    const f32x4 give0 = th ? kq4[0] : kq4[2], give1 = th ? kq4[1] : kq4[3], keep0 = th ? kq4[2] : kq4[0], keep1 = th ? kq4[3] : kq4[1];
    const int el = h * 16 + (r & 15);
    if (r < 16) { ex[((w - 4) * 2) * 32 + el] = give0; ex[((w - 4) * 2 + 1) * 32 + el] = give1; }
    WAITL0();
    lds_mark(lds, C_EX + (w - 4));
    const unsigned pb = WS_KVP + ((c * 16 + ti) * 2 + hh) * KVB;
    {
      const f32x4 za = (r & 1) ? kq4[1] : kq4[0], zb = (r & 1) ? kq4[3] : kq4[2], zq = (r & 2) ? zb : za;
      __builtin_amdgcn_raw_buffer_store_b128(__builtin_bit_cast(u32x4, zq), rs, (r & 28) == 16 ? pb + 2048 + th * 128 + h * 64 + (r & 3) * 16 : 0x40000000u, 0, SC1);
    }
    lds_wait_ge(lds, C_EX + ((w - 4) ^ 1), 1);
    const unsigned po = r < 16 ? pb + th * 1024 + el * 16 : 0x40000000u;
    __builtin_amdgcn_raw_buffer_store_b128(__builtin_bit_cast(u32x4, keep0 + ex[(((w - 4) ^ 1) * 2) * 32 + el]), rs, po, 0, SC1);
    __builtin_amdgcn_raw_buffer_store_b128(__builtin_bit_cast(u32x4, keep1 + ex[(((w - 4) ^ 1) * 2 + 1) * 32 + el]), rs, po + 512, 0, SC1);
    SB();
    u32x4 h0 = geglu8(st.acc[1], rstd[0]), h1 = geglu8(st.acc[3], rstd[1]);
    asm volatile("" : "+v"(h0), "+v"(h1));
    put_h(h0, 0, cg + 2);
    put_h(h1, 1, cg + 2);
    if (cg + 2 >= jlo) WAITV(2); else WAITV(0);
    publish(lds, C_KV, 4, ws, WS_FKV + (c * 16 + ti) * FS, tag64, lane);
    __builtin_amdgcn_s_setprio(0);
    if (!isS) {
      bf16x8 pre[13][2];
      d_pre(IntC<13>{}, pre);
      WAITV(0);
      publish(lds, C_H, 8, ws, WS_FH + (ti * 16 + fw) * FS, tag64, lane);
      lds_wait_ge(lds, C_R1 + (w ^ 1), 1);
      d_refill(13, 0, 13);
      d_phase(IntC<26>{}, IntC<13>{}, BoolC<false>{}, pre);
    }
  } else {
    auto w2c = [&](int k) {
      const int L = (w >> 1) + 2 * k;
      glds16(wo + (6 + L) * 16384 + th * 8192, wl, lb + L_SB + L * 2048 + th * 1024);
    };
    u32x4 h0 = geglu8(st.acc[0], rstd[0]);
    asm volatile("" : "+v"(h0));
    WAITV(0);
    lds_mark(lds, C_P);
    put_h(h0, 0, 2 * cg);
    put_h(geglu8(st.acc[1], rstd[0]), 0, 2 * cg + 1);
    put_h(geglu8(st.acc[2], rstd[1]), 1, 2 * cg);
    put_h(geglu8(st.acc[3], rstd[1]), 1, 2 * cg + 1);
    SB();
    bf16x8 qf0 = pack_regs_scaled<0>(st.acc[4], rstd[0]), qf1 = pack_regs_scaled<8>(st.acc[4], rstd[0]);
    asm volatile("" : "+v"(qf0), "+v"(qf1));
    bf16x8 pre[14][2];
    d_pre(IntC<14>{}, pre);
    bf16x8 ph0[2], ph1[2];
    const f32x16 p0 = MFMA(st.wf[0], qf0, zero);
    dual_softmax(p0, ph0);
    asm volatile("" : "+v"(ph0[0]), "+v"(ph0[1]));
    WAITV(0);
    publish(lds, C_H, 8, ws, WS_FH + (ti * 16 + fw) * FS, tag64, lane);
    const f32x16 p1 = MFMA(st.wf[1], qf1, zero);
    lds_wait_ge(lds, C_R1 + (w ^ 1), 1);
    d_refill(16, 0, 14);
    dual_softmax(p1, ph1);
    LAS bf16x8* pq = (LAS bf16x8*)(lds + L_SCR + (2 * th + u0) * 4096 + lane * 16);
    pq[0] = ph0[0]; pq[64] = ph0[1]; pq[128] = ph1[0]; pq[192] = ph1[1];
    WAITL0();
    lds_mark(lds, C_PQ);
#pragma unroll
    for (int k = 0; k < 8; ++k) w2c(k);
    d_phase(IntC<30>{}, IntC<14>{}, BoolC<true>{}, pre);
  }
  if (!LO && isS) {
    WAITV(0);
    publish(lds, C_H, 8, ws, WS_FH + (ti * 16 + fw) * FS, tag64, lane);
    poll16(ws, WS_FKV + c * 16 * FS, 0, 15, tag64, lane);
    const unsigned go = WS_KVP + (c * 32 + th) * KVB;
    u32x4 pv[16][2], pz[4];
#pragma unroll
    for (int q = 0; q < 4; ++q) pz[q] = __builtin_amdgcn_raw_buffer_load_b128(rs, go + 2048 + (lane >> 4) * (2 * KVB) + (lane & 15) * 16, q * (8 * KVB), SC1);
#pragma unroll
    for (int t = 0; t < 16; ++t)
#pragma unroll
      for (int q = 0; q < 2; ++q) pv[t][q] = __builtin_amdgcn_raw_buffer_load_b128(rs, go + lane * 16 + q * 1024, t * (2 * KVB), SC1);
    SB();
    lds_mark(lds, C_G);
    LAS char* kvf = lds + L_KVF + th * 2048;
    {
      f32x4 z = (__builtin_bit_cast(f32x4, pz[0]) + __builtin_bit_cast(f32x4, pz[1])) + (__builtin_bit_cast(f32x4, pz[2]) + __builtin_bit_cast(f32x4, pz[3]));
#pragma unroll
      for (int e = 0; e < 4; ++e) {
        float v = dpp_add<0x128>(z[e]);
        auto rr = __builtin_amdgcn_permlane16_swap(asu(v), asu(v), false, false);
        const unsigned r0 = rr[0], r1 = rr[1];
        z[e] = add_x32(asf(r0) + asf(r1));
      }
      *(LAS u32x2*)(kvf + ((lane >> 1) & 1) * 1024 + (lane & 1) * 8 + (((lane >> 2) & 1) * 32 + 16) * 16) = u32x2{pk2(z[0], z[1]), pk2(z[2], z[3])};
    }
#pragma unroll
    for (int q = 0; q < 2; ++q) {
      f32x4 a0 = __builtin_bit_cast(f32x4, pv[0][q]);
#pragma unroll
      for (int t = 1; t < 16; ++t) a0 += __builtin_bit_cast(f32x4, pv[t][q]);
      *(LAS u32x2*)(kvf + q * 1024 + (((lane >> 4) & 1) * 32 + (lane & 15)) * 16 + h * 8) = u32x2{pk2(a0[0], a0[1]), pk2(a0[2], a0[3])};
    }
    WAITL0();
    LAS char* kvr = lds + L_KVF + th * 2048 + (h * 32 + min(r, 16)) * 16;
    const bf16x8 k0 = lds_rd128(kvr), k1 = lds_rd128(kvr + 1024);
    lds_wait_ge(lds, C_PQ, 4);
    u32x4 of[4];
#pragma unroll
    for (int t = 0; t < 4; ++t) {
      LAS char* pq = lds + L_SCR + (t * 2 + th) * 2048 + lane * 16;
      f32x16 nm = MFMA(k0, lds_rd128(pq), zero);
      nm = MFMA(k1, lds_rd128(pq + 1024), nm);
      of[t] = __builtin_bit_cast(u32x4, pack_regs_scaled<0>(nm, __builtin_amdgcn_rcpf(nm[8] + 1e-6f)));
    }
    asm volatile("" : "+v"(of[0]), "+v"(of[1]), "+v"(of[2]), "+v"(of[3]));
#pragma unroll
    for (int t = 0; t < 4; ++t) __builtin_amdgcn_raw_buffer_store_b128(of[t], rs, WS_O + ((4 * ti + t) * 32 + 2 * c + th) * 1024 + lane * 16, 0, SC1);
    SB();
    lds_mark(lds, C_R1 + w);
    WAITV(0);
    publish(lds, C_O, 2, ws, WS_FO + (ti * 16 + c) * FS, tag64, lane);
    lds_wait_ge(lds, C_R1 + (w ^ 1), 1);
#pragma unroll
    for (int k = 0; k < 10; ++k) glds16(wo + (22 + k) * 16384 + tg * 8192, wl, lb + L_SCR + k * 2048 + tg * 1024);
    WAITV(0);
    lds_mark(lds, C_WS);
  }
}


__global__ __launch_bounds__(512, 2) void fused_block(const char* x, const char* wqkv, const char* wphi, const char* wo, const char* wgate,
                                                      const char* wup, const char* wdown, char* out, char* ws) {
  extern __shared__ __attribute__((aligned(16))) char smem[];
  asm volatile("" : "+s"(x), "+s"(wqkv), "+s"(wphi), "+s"(wo), "+s"(wgate), "+s"(wup), "+s"(wdown), "+s"(out), "+s"(ws));
  const int w = __builtin_amdgcn_readfirstlane(threadIdx.x >> 6);
  const int lane = threadIdx.x & 63, xcd = blockIdx.x & 7, idx = blockIdx.x >> 3;
  const int ti = 8 * (xcd & 1) + (idx & 7), c = 4 * (xcd >> 1) + (idx >> 3);
  const int fw = c < SLOWC ? c : (c < SLOWC + 2 ? c - SLOWC + 14 : c - 2), f0 = min(96 * fw, FF - 96), gst = fw < 15 ? 128 * FF : 0;
  const unsigned lb = (unsigned)(size_t)(LAS char*)smem;
  const char* xg = x + (size_t)ti * 128 * 1024;
  Pro pr;
#pragma unroll
  for (int a = 0; a < 2; ++a) {
    const int row = 8 * (2 * w + a) + (lane >> 3);
    pr.xo[a] = row * 1024 + (((lane & 7) ^ ((row >> 1) & 7)) << 4);
    glds16(xg, pr.xo[a], lb + (2 * w + a) * 1024);
  }
  SB();
#pragma unroll
  for (int a = 0; a < 2; ++a) {
    const int p = a == 0 ? w : 8 + (w & 3);
    const int q = 64 * p + lane, rr = q / 12, cc = q - rr * 12;
    pr.go[a] = fw < 15 ? rr * (2 * FF) + (fw < 14 ? cc : max(cc, 8)) * 16 : 0;
    const int mat = cc < 8 ? 1 + ((cc >> 1) & 1) : 0, hh = cc < 8 ? cc >> 2 : (cc - 8) >> 1;
    pr.qo[a] = rr * 3072 + mat * 1024 + hh * 32 + (cc & 1) * 16;
  }
#pragma unroll
  for (int s = 0; s < 2; ++s) {
    const unsigned b = lb + s * STG, p2 = (8 + (w & 3)) * 1024;
    const char *pg = wgate + 2 * f0 + s * gst, *pu = wup + 2 * f0 + s * gst, *pq = wqkv + c * 64 + s * (64 * 3072);
    if (s) {
      glds16(xg + s * 128, pr.xo[0], b + 2 * w * 1024);
      glds16(xg + s * 128, pr.xo[1], b + (2 * w + 1) * 1024);
    }
    glds16(pg, pr.go[0], b + GOFF + w * 1024);
    glds16(pu, pr.go[0], b + UOFF + w * 1024);
    glds16(pq, pr.qo[0], b + QOFF + w * 1024);
    glds16(w < 4 ? pg : pu, pr.go[1], b + (w < 4 ? GOFF : UOFF) + p2);
    if (w >= 4) glds16(pq, pr.qo[1], b + QOFF + p2);
    if (s == 0) {
      if (w == 0) warm64((const char*)__builtin_amdgcn_s_getpc(), min((idx % 5) * 8192 + lane * 128, 40000), lb + L_SINK);
      if (w == 1) glds16(wphi + c * 1024, lane * 16, lb + L_WPH);
      if (w == 2) ((LAS unsigned*)((LAS char*)smem + L_CNT))[lane] = 0u;
      BAR();
    }
  }
  if (w != 0) {
    const char* wp = w >= 4 ? xg + (8 * c + 2 * (w - 4)) * 1024 : (w == 1 ? wqkv + blockIdx.x * 6144 : (w == 2 ? wgate : wup) + blockIdx.x * (4 * FF));
    warm64(wp, min(lane * 128, w >= 4 ? 2044 : (w == 1 ? 6140 : 4 * FF - 4)), lb + L_SINK);
  }
  if (w < 4) run_wave<true>(x, wqkv, wo, wgate, wup, wdown, out, ws, (LAS char*)smem, w, pr);
  else run_wave<false>(x, wqkv, wo, wgate, wup, wdown, out, ws, (LAS char*)smem, w, pr);
}

extern "C" void kernel_launch(void* const* d_in, const int* in_sizes, int n_in, void* d_out, int out_size, void* d_ws, size_t ws_size,
                              hipStream_t stream) {
  (void)hipFuncSetAttribute((const void*)fused_block, hipFuncAttributeMaxDynamicSharedMemorySize, LDS_BYTES);
  fused_block<<<256, 512, LDS_BYTES, stream>>>((const char*)d_in[0], (const char*)d_in[1], (const char*)d_in[2], (const char*)d_in[3],
                                               (const char*)d_in[5], (const char*)d_in[6], (const char*)d_in[7], (char*)d_out, (char*)d_ws);
}
```
